```python
import torch

def _softmax(x, axis=-1):
    return torch.softmax(x, dim=axis)
def _gelu(x):
    return torch.nn.functional.gelu(x, approximate="tanh")
def _silu(x):
    return torch.nn.functional.silu(x)
def _sigmoid(x):
    return torch.sigmoid(x)
def _layernorm(x, axis=-1, eps=1e-5):
    m = x.mean(dim=axis, keepdim=True)
    v = x.var(dim=axis, keepdim=True, unbiased=False)
    return (x - m) / torch.sqrt(v + eps)
def _scatter_add(tgt, idx, src, axis=0):
    return tgt.clone().index_add_(axis, idx, src)
def _segment_sum(src, seg, n):
    out = torch.zeros((n,) + src.shape[1:], dtype=src.dtype)
    return out.index_add_(0, seg, src)
def _pad(x, pw):
    flat = tuple(v for pair in reversed(pw) for v in pair)
    return torch.nn.functional.pad(x, flat)

kernel_name = 'sequential_preLN__gqa_block_cached__geglu_ffn__decode'

def setup_inputs():

    _w = (lambda a: (lambda t: t.to(torch.bfloat16) if t.is_floating_point() else t)(torch.as_tensor(a)))
    def _A_setup():
        B, Sq, D = 2, 1, 640
        Hq, Hkv, Sk, Dh = 32, 32, 8192, 128
        assert Hq % Hkv == 0, (Hq, Hkv)
        return {
            "x": _w((lambda *s: torch.randn(*s, dtype=torch.bfloat16) * 0.05)(B, Sq, D)),
            "k_cache": _w((lambda *s: torch.randn(*s, dtype=torch.bfloat16) * 3.2)(B, Hkv, Sk, Dh)),
            "v_cache": _w((lambda *s: torch.randn(*s, dtype=torch.bfloat16) * 0.4)(B, Hkv, Sk, Dh)),
            "wq": _w((lambda *s: torch.randn(*s, dtype=torch.bfloat16) * 3.2)(D, Hq * Dh) * (D ** -0.5)),
            "wo": _w((lambda *s: torch.randn(*s, dtype=torch.bfloat16) * 0.4)(Hq * Dh, D) * ((Hq * Dh) ** -0.5)),
        }
    _A_in = _A_setup()

    def _B_setup():
        B, S, D, F = 2, 1, 640, 1728
        return {
            "x": _w((lambda *s: torch.randn(*s, dtype=torch.bfloat16) * 0.05)(B, S, D)),
            "w_gate": _w((lambda *s: torch.randn(*s, dtype=torch.bfloat16) * 0.05)(D, F)),
            "w_up": _w((lambda *s: torch.randn(*s, dtype=torch.bfloat16) * 0.05)(D, F)),
            "w_down": _w((lambda *s: torch.randn(*s, dtype=torch.bfloat16) * 0.05)(F, D)),
        }
    _B_in = _B_setup()

    return {
        "A_x": _A_in["x"],
        "A_k_cache": _A_in["k_cache"],
        "A_v_cache": _A_in["v_cache"],
        "A_wq": _A_in["wq"],
        "A_wo": _A_in["wo"],
        "B_x": _B_in["x"],
        "B_w_gate": _B_in["w_gate"],
        "B_w_up": _B_in["w_up"],
        "B_w_down": _B_in["w_down"],
    }

def reference(A_x, A_k_cache, A_v_cache, A_wq, A_wo, B_x, B_w_gate, B_w_up, B_w_down):
    def _A_ref(x, k_cache, v_cache, wq, wo):
        B, Sq, D = x.shape
        Hq, Dh = 32, 128
        Hkv = k_cache.shape[1]
        rep = Hq // Hkv
        q = (lambda x, *d: x.permute(*d))((x @ wq).reshape(B, Sq, Hq, Dh), 0, 2, 1, 3)
        k = (lambda x, n, axis: torch.repeat_interleave(x, n, dim=axis))(k_cache, rep, axis=1)
        v = (lambda x, n, axis: torch.repeat_interleave(x, n, dim=axis))(v_cache, rep, axis=1)
        scores = torch.einsum("bhqd,bhkd->bhqk", q, k) * (Dh ** -0.5)
        p = _softmax(scores, axis=-1)
        o = torch.einsum("bhqk,bhkd->bhqd", p, v)
        return (lambda x, *d: x.permute(*d))(o, 0, 2, 1, 3).reshape(B, Sq, Hq * Dh) @ wo

    def _B_ref(x, w_gate, w_up, w_down):
        return (_gelu(x @ w_gate) * (x @ w_up)) @ w_down

    x = A_x
    h = x + _A_ref(_layernorm(x, axis=-1), A_k_cache, A_v_cache, A_wq, A_wo)
    h2 = h + _B_ref(_layernorm(h, axis=-1), B_w_gate, B_w_up, B_w_down)
    return h2
```

```cpp
#include <hip/hip_runtime.h>
#include <hip/hip_bf16.h>
#include <stdint.h>
#define MIMIC 1
typedef unsigned short u16;
typedef unsigned u32x4 __attribute__((ext_vector_type(4)));
typedef __bf16 bf16x2 __attribute__((ext_vector_type(2)));
constexpr int B_ = 2, D = 640, H = 32, SK = 8192, DH = 128, F = 1728;
constexpr int NSPLIT = 8;

__device__ __forceinline__ float bf2f(u16 x) { return __uint_as_float(((unsigned)x) << 16); }
__device__ __forceinline__ float rbf(float x) {
  return (float)(__bf16)x;
}
__device__ __forceinline__ float rb(float x) {
  return rbf(x);
}
__device__ __forceinline__ u16 f2bf(float x) { __bf16 h = (__bf16)x; return __builtin_bit_cast(u16, h); }

__device__ float block_sum(float v, float* sh) {
  for (int o = 32; o > 0; o >>= 1) v += __shfl_xor(v, o);
  int w = threadIdx.x >> 6, nw = blockDim.x >> 6;
  __syncthreads();
  if ((threadIdx.x & 63) == 0) sh[w] = v;
  __syncthreads();
  float s = 0; for (int i = 0; i < nw; ++i) s += sh[i];
  return s;
}
__device__ void ln640(float* xs, float* sh) {
  float s = 0; for (int i = threadIdx.x; i < D; i += blockDim.x) s += xs[i];
  float mean = block_sum(s, sh) / D;
  float v = 0; for (int i = threadIdx.x; i < D; i += blockDim.x) { float d = xs[i] - mean; v += d * d; }
  float var = block_sum(v, sh) / D;
  float m = rb(mean), vr = rb(var);
  float den = rb(sqrtf(rb(vr + 1e-5f)));
  __syncthreads();
  for (int i = threadIdx.x; i < D; i += blockDim.x) xs[i] = rb(rb(xs[i] - m) / den);
  __syncthreads();
}

__global__ void k_q(const u16* x, const u16* wq, float* q) {
  __shared__ float xs[D]; __shared__ float sh[16];
  int b = blockIdx.y, h = blockIdx.x;
  for (int i = threadIdx.x; i < D; i += blockDim.x) xs[i] = bf2f(x[b * D + i]);
  __syncthreads();
  ln640(xs, sh);
  int d = threadIdx.x;
  float a = 0;
  for (int k = 0; k < D; ++k) a += xs[k] * bf2f(wq[(size_t)k * (H * DH) + h * DH + d]);
  q[(b * H + h) * DH + d] = rb(a);
}

__global__ void __launch_bounds__(256) k_attn(const u16* kc, const u16* vc, const float* q, float* pm, float* pl, float* po) {
  __shared__ float sm[4], sl[4], so[4][DH];
  int s = blockIdx.x, bh = blockIdx.y;
  int lane = threadIdx.x & 63, w = threadIdx.x >> 6, r = lane >> 4, c = lane & 15;
  const int KPS = SK / NSPLIT;
  float qf[8];
  for (int e = 0; e < 8; ++e) qf[e] = q[bh * DH + 8 * c + e];
  const u16* kb = kc + ((size_t)bh * SK + s * KPS) * DH;
  const u16* vb = vc + ((size_t)bh * SK + s * KPS) * DH;
  float m = -INFINITY, l = 0, acc[8] = {0,0,0,0,0,0,0,0};
  for (int k0 = w * 16; k0 < KPS; k0 += 64) {
    u32x4 K[4], V[4];
    for (int t = 0; t < 4; ++t) { K[t] = __builtin_nontemporal_load((const u32x4*)(kb + (size_t)(k0 + 4 * t + r) * DH + 8 * c)); V[t] = __builtin_nontemporal_load((const u32x4*)(vb + (size_t)(k0 + 4 * t + r) * DH + 8 * c)); }
    float sc[4];
    for (int t = 0; t < 4; ++t) {
      float d = 0;
      for (int j = 0; j < 4; ++j) { d += __uint_as_float(K[t][j] << 16) * qf[2 * j]; d += __uint_as_float(K[t][j] & 0xffff0000u) * qf[2 * j + 1]; }
      for (int o = 1; o < 16; o <<= 1) d += __shfl_xor(d, o);
      sc[t] = rb(rb(d) * 0.08838834764831845f);
    }
    float mx = fmaxf(fmaxf(sc[0], sc[1]), fmaxf(sc[2], sc[3]));
    mx = fmaxf(mx, __shfl_xor(mx, 16)); mx = fmaxf(mx, __shfl_xor(mx, 32));
    float mn = fmaxf(m, mx), al = __expf(m - mn);
    m = mn; l *= al; for (int e = 0; e < 8; ++e) acc[e] *= al;
    for (int t = 0; t < 4; ++t) {
      float p = __expf(sc[t] - mn); l += p;
      for (int j = 0; j < 4; ++j) { acc[2 * j] += p * __uint_as_float(V[t][j] << 16); acc[2 * j + 1] += p * __uint_as_float(V[t][j] & 0xffff0000u); }
    }
  }
  l += __shfl_xor(l, 16); l += __shfl_xor(l, 32);
  for (int e = 0; e < 8; ++e) { acc[e] += __shfl_xor(acc[e], 16); acc[e] += __shfl_xor(acc[e], 32); }
  if (r == 0) { for (int e = 0; e < 8; ++e) so[w][8 * c + e] = acc[e]; if (c == 0) { sm[w] = m; sl[w] = l; } }
  __syncthreads();
  if (threadIdx.x < DH) {
    float M = fmaxf(fmaxf(sm[0], sm[1]), fmaxf(sm[2], sm[3])), L = 0, o = 0;
    for (int i = 0; i < 4; ++i) { float wt = __expf(sm[i] - M); L += wt * sl[i]; o += wt * so[i][threadIdx.x]; }
    po[(bh * NSPLIT + s) * DH + threadIdx.x] = o;
    if (threadIdx.x == 0) { pm[bh * NSPLIT + s] = M; pl[bh * NSPLIT + s] = L; }
  }
}

__global__ void k_comb(const float* pm, const float* pl, const float* po, float* o) {
  int bh = blockIdx.x, d = threadIdx.x;
  float M = -INFINITY; for (int s = 0; s < NSPLIT; ++s) M = fmaxf(M, pm[bh * NSPLIT + s]);
  float L = 0, a = 0;
  for (int s = 0; s < NSPLIT; ++s) { float wt = __expf(pm[bh * NSPLIT + s] - M); L += wt * pl[bh * NSPLIT + s]; a += wt * po[(bh * NSPLIT + s) * DH + d]; }
  o[bh * DH + d] = rb(a / L);
}

__global__ void k_wo(const float* o, const u16* wo, const u16* x, float* hbuf) {
  __shared__ float part[16][64];
  int b = blockIdx.y, n = blockIdx.x * 64 + (threadIdx.x & 63), w = threadIdx.x >> 6;
  float a = 0;
  for (int k = w; k < H * DH; k += 16) a += o[b * H * DH + k] * bf2f(wo[(size_t)k * D + n]);
  part[w][threadIdx.x & 63] = a;
  __syncthreads();
  if (w == 0) { float s = 0; for (int i = 0; i < 16; ++i) s += part[i][threadIdx.x]; hbuf[b * D + n] = rb(bf2f(x[b * D + n]) + rb(s)); }
}
__global__ void k_gu(const float* hbuf, const u16* wg, const u16* wu, float* act) {
  __shared__ float xs[D]; __shared__ float sh[16]; __shared__ float pg[16][64], pu[16][64];
  int b = blockIdx.y, n = blockIdx.x * 64 + (threadIdx.x & 63), w = threadIdx.x >> 6;
  for (int i = threadIdx.x; i < D; i += blockDim.x) xs[i] = hbuf[b * D + i];
  __syncthreads();
  ln640(xs, sh);
  float g = 0, u = 0;
  for (int k = w; k < D; k += 16) { g += xs[k] * bf2f(wg[(size_t)k * F + n]); u += xs[k] * bf2f(wu[(size_t)k * F + n]); }
  pg[w][threadIdx.x & 63] = g; pu[w][threadIdx.x & 63] = u;
  __syncthreads();
  if (w == 0) {
    float gs = 0, us = 0; for (int i = 0; i < 16; ++i) { gs += pg[i][threadIdx.x]; us += pu[i][threadIdx.x]; }
    gs = rb(gs); us = rb(us);
    float inner = 0.7978845608028654f * (gs + 0.044715f * gs * gs * gs);
    float ge = rb(0.5f * gs * (1.f + tanhf(inner)));
    act[b * F + n] = rb(ge * us);
  }
}
__global__ void k_down(const float* act, const u16* wd, const float* hbuf, u16* out) {
  __shared__ float part[16][64];
  int b = blockIdx.y, n = blockIdx.x * 64 + (threadIdx.x & 63), w = threadIdx.x >> 6;
  float a = 0;
  for (int k = w; k < F; k += 16) a += act[b * F + k] * bf2f(wd[(size_t)k * D + n]);
  part[w][threadIdx.x & 63] = a;
  __syncthreads();
  if (w == 0) { float s = 0; for (int i = 0; i < 16; ++i) s += part[i][threadIdx.x]; out[b * D + n] = f2bf(hbuf[b * D + n] + rb(s)); }
}

extern "C" void kernel_launch(void* const* d_in, const int* in_sizes, int n_in, void* d_out, int out_size, void* d_ws, size_t ws_size, hipStream_t stream) {
  const u16* x = (const u16*)d_in[0]; const u16* kc = (const u16*)d_in[1]; const u16* vc = (const u16*)d_in[2];
  const u16* wq = (const u16*)d_in[3]; const u16* wo = (const u16*)d_in[4];
  const u16* wg = (const u16*)d_in[6]; const u16* wu = (const u16*)d_in[7]; const u16* wd = (const u16*)d_in[8];
  float* ws = (float*)d_ws;
  float* q = ws; float* pm = q + 8192; float* pl = pm + 1024; float* po = pl + 1024; float* o = po + 64 * NSPLIT * DH; float* hb = o + 8192; float* act = hb + 2048;
  k_q<<<dim3(H, B_), 128, 0, stream>>>(x, wq, q);
  k_attn<<<dim3(NSPLIT, B_ * H), 256, 0, stream>>>(kc, vc, q, pm, pl, po);
  k_comb<<<B_ * H, DH, 0, stream>>>(pm, pl, po, o);
  k_wo<<<dim3(D / 64, B_), 1024, 0, stream>>>(o, wo, x, hb);
  k_gu<<<dim3(F / 64, B_), 1024, 0, stream>>>(hb, wg, wu, act);
  k_down<<<dim3(D / 64, B_), 1024, 0, stream>>>(act, wd, hb, (u16*)d_out);
}
```

```cpp
#include <hip/hip_runtime.h>
#include <stdint.h>

#define DIAG 0

namespace blk {
typedef unsigned u32;
typedef unsigned long long u64;
typedef u32 u32x2 __attribute__((ext_vector_type(2)));
typedef u32 u32x4 __attribute__((ext_vector_type(4)));
typedef float f32x4 __attribute__((ext_vector_type(4)));
typedef __bf16 bf16x2 __attribute__((ext_vector_type(2)));
typedef __bf16 bf16x8 __attribute__((ext_vector_type(8)));
typedef __amdgpu_buffer_rsrc_t rsrc_t;

constexpr int DM = 640, NH = 32, SK = 8192, DH = 128, FF = 1728;
constexpr int NB = 4;
constexpr int RPW = 128;
constexpr u32 SENT = 0xFFFFFFFFu;
constexpr u32 OOB = 0x7FFFFFF0u;
constexpr u32 SPIN_CAP = 1u << 21;
constexpr float LOG2E = 1.4426950408889634f;
constexpr float SCALE = 0.08838834764831845f;
constexpr u32 WS_EPOCH = 0, WS_FLAG = 4096, WS_QG = 8192, WS_R = WS_QG + 256 * 2048, WS_Y = WS_R + 256 * 640,
              WS_FP = WS_Y + 2 * 8 * 2560, WS_END = WS_FP + 108 * 2560, WS_DIAG = 2u << 20;
constexpr int AUX_NT = 2, AUX_SC1 = 16;
constexpr int L_QP = 0;
constexpr int L_ML = 2048;
constexpr int L_QS = 2064;
constexpr int L_CNT = 2128;
constexpr int L_OS = 2132;
constexpr int L_ACT = 2644;
constexpr int L_END = 2692;

__device__ __forceinline__ rsrc_t mk(const void* p, u32 bytes) { return __builtin_amdgcn_make_buffer_rsrc((void*)p, 0, bytes, 0x00020000); }
template <int AUX> __device__ __forceinline__ u32x4 ld16(rsrc_t r, u32 v, u32 s) { return __builtin_amdgcn_raw_buffer_load_b128(r, v, s, AUX); }
template <int AUX> __device__ __forceinline__ u32x2 ld8(rsrc_t r, u32 v, u32 s) { return __builtin_amdgcn_raw_buffer_load_b64(r, v, s, AUX); }
__device__ __forceinline__ void st16(rsrc_t r, u32 v, u32x4 x) { __builtin_amdgcn_raw_buffer_store_b128(x, r, v, 0, AUX_SC1); }
__device__ __forceinline__ void st8(rsrc_t r, u32 v, u32x2 x) { __builtin_amdgcn_raw_buffer_store_b64(x, r, v, 0, AUX_SC1); }
__device__ __forceinline__ float asf(u32 x) { return __builtin_bit_cast(float, x); }
__device__ __forceinline__ u32 asu(float x) { return __builtin_bit_cast(u32, x); }
__device__ __forceinline__ float blo(u32 x) { return asf(x << 16); }
__device__ __forceinline__ float bhi(u32 x) { return asf(x & 0xffff0000u); }
__device__ __forceinline__ u32 pkbf(float a, float b) { bf16x2 p = {(__bf16)a, (__bf16)b}; return __builtin_bit_cast(u32, p); }
__device__ __forceinline__ float rbf(float x) { return (float)(__bf16)x; }
__device__ __forceinline__ float dot2(u32 a, u32 b, float c) { return __builtin_amdgcn_fdot2_f32_bf16(__builtin_bit_cast(bf16x2, a), __builtin_bit_cast(bf16x2, b), c, false); }
__device__ __forceinline__ f32x4 mfma(u32x4 a, u32x4 b, f32x4 c) { return __builtin_amdgcn_mfma_f32_16x16x32_bf16(__builtin_bit_cast(bf16x8, a), __builtin_bit_cast(bf16x8, b), c, 0, 0, 0); }
template <int C> __device__ __forceinline__ float dppf(float x) { return asf(__builtin_amdgcn_mov_dpp(asu(x), C, 0xf, 0xf, true)); }
template <int C> __device__ __forceinline__ u32 dppu(u32 x) { return __builtin_amdgcn_mov_dpp(x, C, 0xf, 0xf, true); }
constexpr int XOR1 = 0xB1, XOR2 = 0x4E, XOR7 = 0x141, ROR8 = 0x128, ROR4 = 0x124;
__device__ __forceinline__ float sum16(float x) { auto s = __builtin_amdgcn_permlane16_swap(asu(x), asu(x), false, false); return asf(s[0]) + asf(s[1]); }
__device__ __forceinline__ float sum32(float x) { auto s = __builtin_amdgcn_permlane32_swap(asu(x), asu(x), false, false); return asf(s[0]) + asf(s[1]); }
__device__ __forceinline__ float max16(float x) { auto s = __builtin_amdgcn_permlane16_swap(asu(x), asu(x), false, false); return fmaxf(asf(s[0]), asf(s[1])); }
__device__ __forceinline__ float max32(float x) { auto s = __builtin_amdgcn_permlane32_swap(asu(x), asu(x), false, false); return fmaxf(asf(s[0]), asf(s[1])); }
__device__ __forceinline__ float rowsum(float x) { x += dppf<XOR1>(x); x += dppf<XOR2>(x); x += dppf<XOR7>(x); x += dppf<ROR8>(x); return x; }
__device__ __forceinline__ u32 clean(float x) { u32 u = asu(x); return u == SENT ? 0x7FC00000u : u; }
__device__ __forceinline__ u32x4 clean4(f32x4 v) { return u32x4{clean(v[0]), clean(v[1]), clean(v[2]), clean(v[3])}; }
__device__ __forceinline__ bool full4(u32x4 v) { return (v[0] != SENT) & (v[1] != SENT) & (v[2] != SENT) & (v[3] != SENT); }
__device__ __forceinline__ f32x4 asf4(u32x4 v) { return __builtin_bit_cast(f32x4, v); }
__device__ __forceinline__ float ex2(float x) { return __builtin_amdgcn_exp2f(x); }
#define FENCE() asm volatile("" ::: "memory")
#define BAR() do { asm volatile("s_waitcnt lgkmcnt(0)" ::: "memory"); __builtin_amdgcn_s_barrier(); asm volatile("" ::: "memory"); } while (0)
#define SB() __builtin_amdgcn_sched_barrier(0)
#define STAMP(n) do { } while (0)

__device__ __forceinline__ void transpose8(const u32x4 (&in)[8], u32x4 (&out)[8]) {
#pragma unroll
  for (int k = 0; k < 4; ++k)
#pragma unroll
    for (int d = 0; d < 4; ++d) {
      out[2 * d][k] = __builtin_amdgcn_perm(in[2 * k + 1][d], in[2 * k][d], 0x05040100u);
      out[2 * d + 1][k] = __builtin_amdgcn_perm(in[2 * k + 1][d], in[2 * k][d], 0x07060302u);
    }
#pragma unroll
  for (int e = 0; e < 8; ++e) asm volatile("" : "+v"(out[e]));
}

__global__ void __launch_bounds__(512, 2)
k_block(const void* __restrict__ px, const void* __restrict__ pk, const void* __restrict__ pv, const void* __restrict__ pwq,
        const void* __restrict__ pwo, const void* __restrict__ pwg, const void* __restrict__ pwu, const void* __restrict__ pwd,
        void* __restrict__ pout, void* __restrict__ pws) {
  __shared__ __attribute__((aligned(16))) u32 lds[L_END];
  float* const ldf = reinterpret_cast<float*>(lds);
  const int j = blockIdx.x, xcd = j & 7, ii = j >> 3;
  const int tid = threadIdx.x, lane = tid & 63, w = __builtin_amdgcn_readfirstlane(tid >> 6);
  const int r = lane >> 4, c = lane & 15;
  const rsrc_t rws = mk(pws, WS_END);
  STAMP(0);
  const int bh = (ii & 15) * 4 + (xcd >> 1), sp = (xcd & 1) + 2 * (ii >> 4);
  const int gn = ii - 10, gm = xcd + 8 * (gn >> 2);
  const bool isW = ii < 10, isG = gn >= 0 && gn < 16 && gm < 27, isF = ii >= 26 && ii < 31;
  const int gb = gn & 1, gc = 2 * gm + ((gn >> 1) & 1);
  const rsrc_t rx = mk(px, 2 * DM * 2);
  const u32 kvbase = (u32)(bh * SK + sp * 2048) * 256u;
  const u32 vo = lane * 16;
  u32x4 KV[NB][8];
  bool val[NB];
#define ISSUE(b, idx) do { const u32 nb_ = val[b] ? (u32)(2 * NH * SK * DH * 2) : 0u; const rsrc_t rk_ = mk(pk, nb_), rv_ = mk(pv, nb_); const u32 so_ = kvbase + (u32)(idx) * 4096u; \
    _Pragma("unroll") for (int t_ = 0; t_ < 4; ++t_) KV[b][t_] = ld16<AUX_NT>(rk_, vo + t_ * 1024, so_); \
    _Pragma("unroll") for (int t_ = 0; t_ < 4; ++t_) KV[b][4 + t_] = ld16<AUX_NT>(rv_, vo + t_ * 1024, so_); } while (0)
  u32 tag = 0, tag_hi = 0;
  if (w != 0) {
#pragma unroll
    for (int b = 0; b < NB; ++b) { val[b] = true; ISSUE(b, b * 7 + w - 1); }
    FENCE(); SB();
    STAMP(1);
  } else {
    __builtin_amdgcn_s_setprio(3);
#pragma unroll
    for (int b = 0; b < NB; ++b) val[b] = false;
    const u32x4 s4 = {SENT, SENT, SENT, SENT};
    st16(rws, lane < 33 ? WS_R + (bh * 4 + sp) * 640 + vo : OOB, s4);
    {
      const u32 fa = WS_FP + (gb * 54 + gc) * 2560 + vo;
      st16(rws, isW ? (lane < 32 ? WS_Y + (r * 8 + xcd) * 2560 + ii * 256 + c * 16 : OOB) : (isG ? fa : OOB), s4);
      st16(rws, isG ? fa + 1024 : OOB, s4);
      st16(rws, isG && lane < 32 ? fa + 2048 : OOB, s4);
    }
    FENCE(); SB();
    const int qh = j >> 3, qk = j & 7;
    const u32x2 ep = ld8<AUX_SC1>(rws, WS_EPOCH, 0);
    u32x4 xc[3], xa, xb; u32x2 xd; u32x4 wqv[20];
    {
      const rsrc_t rwq = mk(pwq, DM * NH * DH * 2);
      const u32 hb = (lane >> 5) * 1280 + (lane & 31) * 16;
      xc[0] = ld16<0>(rx, hb, 0); xc[1] = ld16<0>(rx, hb + 512, 0); xc[2] = ld16<0>(rx, (lane & 31) < 16 ? hb + 1024 : OOB, 0);
      const u32 row0 = 80 * qk + 20 * r, xo = ((lane >> 3) & 1) * 1280 + row0 * 2;
      xa = ld16<0>(rx, xo, 0); xb = ld16<0>(rx, xo + 16, 0); xd = ld8<0>(rx, xo + 32, 0);
      const u32 wo_ = row0 * 8192 + qh * 256 + c * 16;
#pragma unroll
      for (int t = 0; t < 20; ++t) wqv[t] = ld16<AUX_NT>(rwq, wo_, t * 8192);
    }
    FENCE(); SB();
    STAMP(1);
    if (lane == 0) lds[L_CNT] = 7 * NB;
    tag = (u32)__builtin_amdgcn_readfirstlane(ep[0]) + 1u;
    tag_hi = (u32)__builtin_amdgcn_readfirstlane(ep[1]) + (tag == 0u ? 1u : 0u);
    float s = 0.f;
#pragma unroll
    for (int k = 0; k < 3; ++k)
#pragma unroll
      for (int d = 0; d < 4; ++d) s += blo(xc[k][d]) + bhi(xc[k][d]);
    s = sum16(rowsum(s));
    const float mean = s * (1.f / DM);
    float v = 0.f;
#pragma unroll
    for (int k = 0; k < 3; ++k) {
      float vk = 0.f;
#pragma unroll
      for (int d = 0; d < 4; ++d) { const float a = blo(xc[k][d]) - mean, b2 = bhi(xc[k][d]) - mean; vk = fmaf(a, a, vk); vk = fmaf(b2, b2, vk); }
      v += (k < 2 || (lane & 31) < 16) ? vk : 0.f;
    }
    v = sum16(rowsum(v));
    const float mb = rbf(mean), rinv = 1.f / rbf(sqrtf(rbf(rbf(v * (1.f / DM)) + 1e-5f)));
    const float m0 = asf(__builtin_amdgcn_readlane(asu(mb), 0)), m1 = asf(__builtin_amdgcn_readlane(asu(mb), 32));
    const float i0 = asf(__builtin_amdgcn_readlane(asu(rinv), 0)), i1 = asf(__builtin_amdgcn_readlane(asu(rinv), 32));
    const bool b1 = (lane >> 3) & 1;
    const float mm = b1 ? m1 : m0, ri = b1 ? i1 : i0;
    const u32 xw[10] = {xa[0], xa[1], xa[2], xa[3], xb[0], xb[1], xb[2], xb[3], xd[0], xd[1]};
    u32 sl[4], sh[4];
#pragma unroll
    for (int k = 0; k < 4; ++k) {
      const bool on = ((lane & 7) >> 1) == k;
      sl[k] = on ? ((lane & 1) ? 0x01000c0cu : 0x0c0c0100u) : 0x0c0c0c0cu;
      sh[k] = on ? ((lane & 1) ? 0x03020c0cu : 0x0c0c0302u) : 0x0c0c0c0cu;
    }
    f32x4 qa = {0.f, 0.f, 0.f, 0.f}, qb2 = {0.f, 0.f, 0.f, 0.f};
#pragma unroll
    for (int t = 0; t < 10; ++t) {
      const u32 dp = pkbf(blo(xw[t]) - mm, bhi(xw[t]) - mm);
      const u32 xn = pkbf(blo(dp) * ri, bhi(dp) * ri);
      qa = mfma(u32x4{__builtin_amdgcn_perm(xn, xn, sl[0]), __builtin_amdgcn_perm(xn, xn, sl[1]), __builtin_amdgcn_perm(xn, xn, sl[2]), __builtin_amdgcn_perm(xn, xn, sl[3])}, wqv[2 * t], qa);
      qb2 = mfma(u32x4{__builtin_amdgcn_perm(xn, xn, sh[0]), __builtin_amdgcn_perm(xn, xn, sh[1]), __builtin_amdgcn_perm(xn, xn, sh[2]), __builtin_amdgcn_perm(xn, xn, sh[3])}, wqv[2 * t + 1], qb2);
    }
    qa += qb2;
    FENCE();
    st16(rws, WS_QG + j * 2048 + vo, u32x4{asu(qa[0]), tag, asu(qa[1]), tag});
    st16(rws, WS_QG + j * 2048 + 1024 + vo, u32x4{asu(qa[2]), tag, asu(qa[3]), tag});
    st8(rws, lane == 0 ? WS_FLAG + j * 8 : OOB, u32x2{tag, tag_hi});
    STAMP(2);
    const u32 qb = WS_QG + (u32)((bh & 31) * 8) * 2048u + (u32)(bh >> 5) * 512u;
    const u32 qo = (lane >> 5) * 1024 + (lane & 31) * 16;
    u32x4 g[8];
    for (u32 spins = 0; spins < SPIN_CAP; ++spins) {
      FENCE();
      bool ok = true;
#pragma unroll
      for (int k = 0; k < 8; ++k) g[k] = ld16<AUX_SC1>(rws, qo, qb + k * 2048);
#pragma unroll
      for (int k = 0; k < 8; ++k) ok &= (g[k][1] == tag) & (g[k][3] == tag);
      if (__all(ok)) break;
    }
    float q0 = 0.f, q1 = 0.f;
#pragma unroll
    for (int k = 0; k < 8; ++k) { q0 += asf(g[k][0]); q1 += asf(g[k][2]); }
    lds[L_QS + 4 * c + 2 * (r & 1) + (r >> 1)] = pkbf(q0, q1);
  }
  BAR();
  STAMP(3);
  const u32x4 qv = *reinterpret_cast<const u32x4*>(lds + L_QS + 4 * c);
  if (w == 0) {
    for (u32 spins = 0; spins < SPIN_CAP; ++spins) {
      FENCE();
      const u32x4 f0 = ld16<AUX_SC1>(rws, WS_FLAG + vo, 0), f1 = ld16<AUX_SC1>(rws, WS_FLAG + 1024 + vo, 0);
      const bool ok = (f0[0] == tag) & (f0[1] == tag_hi) & (f0[2] == tag) & (f0[3] == tag_hi) & (f1[0] == tag) & (f1[1] == tag_hi) & (f1[2] == tag) & (f1[3] == tag_hi);
      if (__all(ok)) break;
      __builtin_amdgcn_s_sleep(4);
    }
    __builtin_amdgcn_s_setprio(0);
  }
  u32 psel[4];
  {
    const u32 src = (lane & 8) ? 0x0302u : 0x0100u;
    const u32 one = (lane & 1) ? ((src << 16) | 0x0c0cu) : (0x0c0c0000u | src);
#pragma unroll
    for (int k = 0; k < 4; ++k) psel[k] = (((lane & 7) >> 1) == k) ? one : 0x0c0c0c0cu;
  }
  const void* pa = pwo; const void* pb = pwo; u32 na = 0, nb2 = 0, sa = 1280, va = 0, vb = 0;
  if (isW) { na = NH * DH * DM * 2; va = (512 * xcd + 64 * w + 8 * (lane >> 3)) * 1280 + ii * 128 + (lane & 7) * 16; }
  if (isG) {
    if (w < 5) { pa = pwg; pb = pwu; na = nb2 = DM * FF * 2; sa = 3456; va = vb = (128 * w + 8 * (lane >> 2)) * 3456 + gc * 64 + (lane & 3) * 16; }
    else { pa = pb = pwd; na = FF * DM * 2; nb2 = w < 7 ? na : 0u; va = (32 * gc + 8 * r) * 1280 + (w - 5) * 512 + c * 16; vb = va + 256; }
  }
  asm volatile("" : "+v"(va), "+v"(vb));
  float m = -INFINITY, l = 0.f;
  f32x4 acc = {0.f, 0.f, 0.f, 0.f};
#define COMPUTE(b) do { float d_[4]; \
    _Pragma("unroll") for (int t_ = 0; t_ < 4; ++t_) { float s_ = dot2(KV[b][t_][0], qv[0], 0.f); s_ = dot2(KV[b][t_][1], qv[1], s_); s_ = dot2(KV[b][t_][2], qv[2], s_); d_[t_] = dot2(KV[b][t_][3], qv[3], s_); } \
    const float a0_ = d_[0] + dppf<ROR8>(d_[0]), a2_ = d_[2] + dppf<ROR8>(d_[2]), a1_ = d_[1] + dppf<ROR8>(d_[1]), a3_ = d_[3] + dppf<ROR8>(d_[3]); \
    const float x0_ = (c & 8) ? a2_ : a0_, x1_ = (c & 8) ? a3_ : a1_; \
    const float y0_ = x0_ + dppf<XOR7>(x0_), y1_ = x1_ + dppf<XOR7>(x1_); \
    float z_ = (c & 4) ? y1_ : y0_; z_ += dppf<XOR2>(z_); z_ += dppf<XOR1>(z_); \
    const float sc_ = rbf(rbf(z_) * SCALE); \
    float mx_ = fmaxf(sc_, dppf<ROR4>(sc_)); mx_ = fmaxf(mx_, dppf<ROR8>(mx_)); mx_ = max32(max16(mx_)); \
    const float mn_ = fmaxf(m, mx_), al_ = ex2((m - mn_) * LOG2E), p_ = ex2((sc_ - mn_) * LOG2E); \
    m = mn_; l = fmaf(l, al_, p_); acc *= al_; \
    const u32 pk_ = pkbf(p_, p_ - rbf(p_)); \
    { const u32 bc_ = dppu<0x150>(pk_); acc = mfma(u32x4{__builtin_amdgcn_perm(bc_, bc_, psel[0]), __builtin_amdgcn_perm(bc_, bc_, psel[1]), __builtin_amdgcn_perm(bc_, bc_, psel[2]), __builtin_amdgcn_perm(bc_, bc_, psel[3])}, KV[b][4], acc); } \
    { const u32 bc_ = dppu<0x154>(pk_); acc = mfma(u32x4{__builtin_amdgcn_perm(bc_, bc_, psel[0]), __builtin_amdgcn_perm(bc_, bc_, psel[1]), __builtin_amdgcn_perm(bc_, bc_, psel[2]), __builtin_amdgcn_perm(bc_, bc_, psel[3])}, KV[b][5], acc); } \
    { const u32 bc_ = dppu<0x158>(pk_); acc = mfma(u32x4{__builtin_amdgcn_perm(bc_, bc_, psel[0]), __builtin_amdgcn_perm(bc_, bc_, psel[1]), __builtin_amdgcn_perm(bc_, bc_, psel[2]), __builtin_amdgcn_perm(bc_, bc_, psel[3])}, KV[b][6], acc); } \
    { const u32 bc_ = dppu<0x15C>(pk_); acc = mfma(u32x4{__builtin_amdgcn_perm(bc_, bc_, psel[0]), __builtin_amdgcn_perm(bc_, bc_, psel[1]), __builtin_amdgcn_perm(bc_, bc_, psel[2]), __builtin_amdgcn_perm(bc_, bc_, psel[3])}, KV[b][7], acc); } \
  } while (0)
  for (;;) {
#pragma unroll
    for (int b = 0; b < NB; ++b) {
      if (val[b]) COMPUTE(b);
      u32 nx = 0;
      if (lane == 0) nx = __hip_atomic_fetch_add(lds + L_CNT, 1u, __ATOMIC_RELAXED, __HIP_MEMORY_SCOPE_WORKGROUP);
      const u32 idx = (u32)__builtin_amdgcn_readfirstlane(nx);
      val[b] = idx < (u32)RPW;
      SB();
      ISSUE(b, idx);
      SB();
    }
    if (!val[NB - 1]) break;
  }
  STAMP(4);
  u32x4 WA[8], WB[8];
  {
    const rsrc_t ra = mk(pa, na), rb = mk(pb, nb2);
#pragma unroll
    for (int t = 0; t < 8; ++t) WA[t] = ld16<AUX_NT>(ra, va, t * sa);
#pragma unroll
    for (int t = 0; t < 8; ++t) WB[t] = ld16<AUX_NT>(rb, vb, t * sa);
  }
  SB();
#pragma unroll
  for (int b = 0; b < NB; ++b) if (val[b]) COMPUTE(b);
  STAMP(5);
  {
    float lt = l + dppf<ROR4>(l); lt += dppf<ROR8>(lt); lt = sum32(sum16(lt));
    const f32x4 a4 = {sum32(acc[0]), sum32(acc[1]), sum32(acc[2]), sum32(acc[3])};
    if (lane < 32) *reinterpret_cast<f32x4*>(ldf + L_QP + w * 128 + lane * 4) = a4;
    if (lane == 0) { ldf[L_ML + 2 * w] = m; ldf[L_ML + 2 * w + 1] = lt; }
  }
  BAR();
  STAMP(6);
  if (w == 0) {
    float mw[8], lw[8];
#pragma unroll
    for (int k = 0; k < 4; ++k) { const f32x4 t4 = *reinterpret_cast<const f32x4*>(ldf + L_ML + 4 * k); mw[2 * k] = t4[0]; lw[2 * k] = t4[1]; mw[2 * k + 1] = t4[2]; lw[2 * k + 1] = t4[3]; }
    float M = mw[0];
#pragma unroll
    for (int k = 1; k < 8; ++k) M = fmaxf(M, mw[k]);
    float L = 0.f, wt[8];
#pragma unroll
    for (int k = 0; k < 8; ++k) { wt[k] = ex2((mw[k] - M) * LOG2E); L = fmaf(wt[k], lw[k], L); }
    const int hf = lane >> 5, ll = lane & 31;
    f32x4 o4 = {0.f, 0.f, 0.f, 0.f};
#pragma unroll
    for (int k = 0; k < 4; ++k) o4 += (hf ? wt[4 + k] : wt[k]) * *reinterpret_cast<const f32x4*>(ldf + L_QP + (4 * hf + k) * 128 + ll * 4);
    o4 = f32x4{sum32(o4[0]), sum32(o4[1]), sum32(o4[2]), sum32(o4[3])};
    if (lane == 32) o4 = f32x4{M, L, 0.f, 0.f};
    const u32 ro = lane < 32 ? (u32)((ll & 15) * 32 + (ll >> 4) * 16) : (lane == 32 ? 512u : OOB);
    st16(rws, lane <= 32 ? WS_R + (bh * 4 + sp) * 640 + ro : OOB, clean4(o4));
  }
  STAMP(7);
  if (isW) {
    u32x4 T[8]; transpose8(WA, T);
    const u32 rb = WS_R + (u32)(((w >> 2) * 32 + 4 * xcd + (w & 3)) * 4) * 640u;
    const u32 va = (lane >> 5) * 640 + (lane & 31) * 16;
    u32x4 a0, a1; u32x2 ml;
    for (u32 spins = 0; spins < SPIN_CAP; ++spins) {
      FENCE();
      a0 = ld16<AUX_SC1>(rws, va, rb); a1 = ld16<AUX_SC1>(rws, va + 1280, rb); ml = ld8<AUX_SC1>(rws, (lane & 3) * 640 + 512, rb);
      if (__all(full4(a0) & full4(a1) & (ml[0] != SENT) & (ml[1] != SENT))) break;
    }
    STAMP(8);
    {
      const float ms = asf(ml[0]), ls = asf(ml[1]);
      float M = fmaxf(ms, dppf<XOR1>(ms)); M = fmaxf(M, dppf<XOR2>(M));
      const float ws_ = ex2((ms - M) * LOG2E);
      float L = ws_ * ls; L += dppf<XOR1>(L); L += dppf<XOR2>(L);
      const float wn = ws_ / L;
      const float w0 = dppf<0x00>(wn), w1 = dppf<0x55>(wn), w2 = dppf<0xAA>(wn), w3 = dppf<0xFF>(wn);
      const bool hf = lane >= 32;
      f32x4 o4 = (hf ? w1 : w0) * asf4(a0) + (hf ? w3 : w2) * asf4(a1);
      o4 = f32x4{sum32(o4[0]), sum32(o4[1]), sum32(o4[2]), sum32(o4[3])};
      if (lane < 32) *reinterpret_cast<u32x2*>(lds + L_OS + (w >> 2) * 256 + (w & 3) * 64 + lane * 2) = u32x2{pkbf(o4[0], o4[1]), pkbf(o4[2], o4[3])};
    }
    BAR();
    {
      const u32x4 af = *reinterpret_cast<const u32x4*>(lds + L_OS + (c >> 3) * 256 + 32 * w + 8 * r + 4 * ((c >> 2) & 1));
      f32x4 dd[8];
#pragma unroll
      for (int e = 0; e < 8; ++e) dd[e] = mfma(af, T[e], f32x4{0.f, 0.f, 0.f, 0.f});
      if ((r & 1) == (c >> 3)) {
        float* pp = ldf + L_QP + ((w * 2 + (r & 1)) * 2 + (r >> 1)) * 64 + 8 * (c & 7);
        *reinterpret_cast<f32x4*>(pp) = f32x4{dd[0][0], dd[1][0], dd[2][0], dd[3][0]};
        *reinterpret_cast<f32x4*>(pp + 4) = f32x4{dd[4][0], dd[5][0], dd[6][0], dd[7][0]};
      }
    }
    BAR();
    if (w == 0) {
      const int hf = lane >> 5;
      f32x4 y4 = {0.f, 0.f, 0.f, 0.f};
#pragma unroll
      for (int k = 0; k < 8; ++k) y4 += *reinterpret_cast<const f32x4*>(ldf + L_QP + (8 * hf + k) * 128 + (lane & 31) * 4);
      y4 = f32x4{sum32(y4[0]), sum32(y4[1]), sum32(y4[2]), sum32(y4[3])};
      st16(rws, lane < 32 ? WS_Y + ((r & 1) * 8 + xcd) * 2560 + ii * 256 + c * 16 : OOB, clean4(y4));
    }
    STAMP(9);
  } else if (isG) {
    u32x4 TA[8], TB[8]; transpose8(WA, TA); transpose8(WB, TB);
    if (w == 7) {
      u32x2 xq[3];
      xq[0] = ld8<0>(rx, gb * 1280 + lane * 8, 0); xq[1] = ld8<0>(rx, gb * 1280 + 512 + lane * 8, 0); xq[2] = ld8<0>(rx, lane < 32 ? gb * 1280 + 1024 + lane * 8 : OOB, 0);
      const u32 yb = WS_Y + gb * 8 * 2560;
      const u32 v2 = lane < 32 ? vo + 2048 : OOB;
      for (u32 spins = 0; spins < SPIN_CAP; ++spins) {
        FENCE();
        if (__all(full4(ld16<AUX_SC1>(rws, vo, yb)))) break;
        __builtin_amdgcn_s_sleep(1);
      }
      u32x4 y[8][3];
      for (u32 spins = 0; spins < SPIN_CAP; ++spins) {
        FENCE();
        bool ok = true;
#pragma unroll
        for (int g = 0; g < 8; ++g) { y[g][0] = ld16<AUX_SC1>(rws, vo, yb + g * 2560); y[g][1] = ld16<AUX_SC1>(rws, vo + 1024, yb + g * 2560); y[g][2] = ld16<AUX_SC1>(rws, v2, yb + g * 2560); }
#pragma unroll
        for (int g = 0; g < 8; ++g) ok &= full4(y[g][0]) & full4(y[g][1]) & full4(y[g][2]);
        if (__all(ok)) break;
      }
      STAMP(8);
      float h[12]; float s = 0.f;
#pragma unroll
      for (int k = 0; k < 3; ++k) {
        const f32x4 t4 = ((asf4(y[0][k]) + asf4(y[1][k])) + (asf4(y[2][k]) + asf4(y[3][k]))) + ((asf4(y[4][k]) + asf4(y[5][k])) + (asf4(y[6][k]) + asf4(y[7][k])));
        h[4 * k] = rbf(blo(xq[k][0]) + rbf(t4[0])); h[4 * k + 1] = rbf(bhi(xq[k][0]) + rbf(t4[1]));
        h[4 * k + 2] = rbf(blo(xq[k][1]) + rbf(t4[2])); h[4 * k + 3] = rbf(bhi(xq[k][1]) + rbf(t4[3]));
        s += (h[4 * k] + h[4 * k + 1]) + (h[4 * k + 2] + h[4 * k + 3]);
      }
      s = sum32(sum16(rowsum(s)));
      const float mean = s * (1.f / DM);
      float v = 0.f, v2s = 0.f;
#pragma unroll
      for (int k = 0; k < 8; ++k) { const float a = h[k] - mean; v = fmaf(a, a, v); }
#pragma unroll
      for (int k = 8; k < 12; ++k) { const float a = h[k] - mean; v2s = fmaf(a, a, v2s); }
      v += lane < 32 ? v2s : 0.f;
      v = sum32(sum16(rowsum(v)));
      const float mb = rbf(mean), rinv = 1.f / rbf(sqrtf(rbf(rbf(v * (1.f / DM)) + 1e-5f)));
#pragma unroll
      for (int k = 0; k < 3; ++k) {
        const u32x2 xn = {pkbf(rbf(h[4 * k] - mb) * rinv, rbf(h[4 * k + 1] - mb) * rinv), pkbf(rbf(h[4 * k + 2] - mb) * rinv, rbf(h[4 * k + 3] - mb) * rinv)};
        if (k < 2 || lane < 32) *reinterpret_cast<u32x2*>(lds + L_OS + k * 128 + lane * 2) = xn;
      }
    }
    BAR();
    if (w < 5) {
      const u32x4 af = *reinterpret_cast<const u32x4*>(lds + L_OS + 64 * w + 16 * r + 4 * (c >> 2));
      f32x4 da[8], db[8];
#pragma unroll
      for (int e = 0; e < 8; ++e) da[e] = mfma(af, TA[e], f32x4{0.f, 0.f, 0.f, 0.f});
#pragma unroll
      for (int e = 0; e < 8; ++e) db[e] = mfma(af, TB[e], f32x4{0.f, 0.f, 0.f, 0.f});
      if (r == (c >> 2)) {
        float* pp = ldf + L_QP + (w * 4 + r) * 64 + 8 * (c & 3);
        *reinterpret_cast<f32x4*>(pp) = f32x4{da[0][0], da[1][0], da[2][0], da[3][0]};
        *reinterpret_cast<f32x4*>(pp + 4) = f32x4{da[4][0], da[5][0], da[6][0], da[7][0]};
        *reinterpret_cast<f32x4*>(pp + 32) = f32x4{db[0][0], db[1][0], db[2][0], db[3][0]};
        *reinterpret_cast<f32x4*>(pp + 36) = f32x4{db[4][0], db[5][0], db[6][0], db[7][0]};
      }
    }
    BAR();
    if (w >= 5) {
      float s0 = 0.f, s1 = 0.f;
#pragma unroll
      for (int k = 0; k < 10; ++k) { s0 += ldf[L_QP + (2 * k) * 64 + lane]; s1 += ldf[L_QP + (2 * k + 1) * 64 + lane]; }
      const float gu = rbf(s0 + s1);
      const auto sw = __builtin_amdgcn_permlane32_swap(asu(gu), asu(gu), false, false);
      const float g = asf(sw[0]), u = asf(sw[1]);
      const float z = 0.7978845608028654f * fmaf(0.044715f * g * g, g, g);
      const float ge = rbf(g / (1.f + ex2(-2.f * LOG2E * z)));
      const float act = ge * u;
      const u32 ap = pkbf(act, dppf<XOR1>(act));
      u32* la = lds + L_ACT + (w - 5) * 16;
      if (lane < 32 && !(lane & 1)) la[lane >> 1] = ap;
      asm volatile("s_waitcnt lgkmcnt(0)" ::: "memory");
      const u32x4 af = *reinterpret_cast<const u32x4*>(la + 4 * r);
      f32x4 da[8], db[8];
#pragma unroll
      for (int e = 0; e < 8; ++e) da[e] = mfma(af, TA[e], f32x4{0.f, 0.f, 0.f, 0.f});
#pragma unroll
      for (int e = 0; e < 8; ++e) db[e] = mfma(af, TB[e], f32x4{0.f, 0.f, 0.f, 0.f});
      f32x4 o4;
#pragma unroll
      for (int v = 0; v < 4; ++v) { const float ea = (r & 1) ? da[4 + v][0] : da[v][0], eb = (r & 1) ? db[4 + v][0] : db[v][0]; o4[v] = (r >> 1) ? eb : ea; }
      const u32 fo = WS_FP + (gb * 54 + gc) * 2560 + (w - 5) * 1024 + (r >> 1) * 512 + c * 32 + (r & 1) * 16;
      st16(rws, (w < 7 || lane < 32) ? fo : OOB, clean4(o4));
    }
    STAMP(9);
  } else if (isF) {
    if (w == 0) {
      const int fu = (ii - 26) * 8 + xcd, fb = fu / 20, fc = fu - 20 * fb;
      const int ug = lane >> 3, q8 = lane & 7;
      const u32x2 xq = ld8<0>(rx, fb * 1280 + fc * 64 + q8 * 8, 0);
      u32x4 y;
      for (u32 spins = 0; spins < SPIN_CAP; ++spins) {
        FENCE();
        y = ld16<AUX_SC1>(rws, WS_Y + (fb * 8 + ug) * 2560 + fc * 128 + q8 * 16, 0);
        if (__all(full4(y))) break;
        __builtin_amdgcn_s_sleep(1);
      }
      f32x4 t4 = asf4(y);
#pragma unroll
      for (int v = 0; v < 4; ++v) t4[v] = sum32(sum16(t4[v] + dppf<ROR8>(t4[v])));
      const float h0 = rbf(blo(xq[0]) + rbf(t4[0])), h1 = rbf(bhi(xq[0]) + rbf(t4[1])), h2 = rbf(blo(xq[1]) + rbf(t4[2])), h3 = rbf(bhi(xq[1]) + rbf(t4[3]));
      STAMP(8);
      const u32 fa = WS_FP + (fb * 54 + ug) * 2560 + fc * 128 + q8 * 16;
      u32x4 p[7];
      for (u32 spins = 0; spins < SPIN_CAP; ++spins) {
        FENCE();
        bool ok = true;
#pragma unroll
        for (int k = 0; k < 7; ++k) p[k] = ld16<AUX_SC1>(rws, (k < 6 || ug < 6) ? fa : OOB, k * 8 * 2560);
#pragma unroll
        for (int k = 0; k < 7; ++k) ok &= full4(p[k]);
        if (__all(ok)) break;
      }
      f32x4 s4 = ((asf4(p[0]) + asf4(p[1])) + (asf4(p[2]) + asf4(p[3]))) + ((asf4(p[4]) + asf4(p[5])) + asf4(p[6]));
#pragma unroll
      for (int v = 0; v < 4; ++v) s4[v] = sum32(sum16(s4[v] + dppf<ROR8>(s4[v])));
      const rsrc_t ro = mk(pout, 2 * DM * 2);
      __builtin_amdgcn_raw_buffer_store_b64(u32x2{pkbf(h0 + rbf(s4[0]), h1 + rbf(s4[1])), pkbf(h2 + rbf(s4[2]), h3 + rbf(s4[3]))}, ro, lane < 8 ? (u32)(fb * 1280 + fc * 64 + q8 * 8) : OOB, 0, 0);
      if (fu == 0) st8(rws, lane == 0 ? WS_EPOCH : OOB, u32x2{tag, tag_hi});
      STAMP(9);
    }
  }
}
}

extern "C" void kernel_launch(void* const* d_in, const int* in_sizes, int n_in, void* d_out, int out_size, void* d_ws, size_t ws_size,
                              hipStream_t stream) {
  hipLaunchKernelGGL(blk::k_block, dim3(256), dim3(512), 0, stream, (const void*)d_in[0], (const void*)d_in[1], (const void*)d_in[2],
                     (const void*)d_in[3], (const void*)d_in[4], (const void*)d_in[6], (const void*)d_in[7], (const void*)d_in[8], d_out, d_ws);
}
```

```cpp
#include <hip/hip_runtime.h>
#include <stdint.h>

#define DIAG 1
#define LEAK 0

namespace blk {
typedef unsigned u32;
typedef unsigned long long u64;
typedef u32 u32x2 __attribute__((ext_vector_type(2)));
typedef u32 u32x4 __attribute__((ext_vector_type(4)));
typedef float f32x4 __attribute__((ext_vector_type(4)));
typedef __bf16 bf16x2 __attribute__((ext_vector_type(2)));
typedef __bf16 bf16x8 __attribute__((ext_vector_type(8)));
typedef __amdgpu_buffer_rsrc_t rsrc_t;

constexpr int DM = 640, NH = 32, SK = 8192, DH = 128, FF = 1728;
constexpr int NB = 4;
constexpr int RPW = 128;
constexpr u32 SENT = 0xFFFFFFFFu;
constexpr u32 OOB = 0x7FFFFFF0u;
constexpr u32 SPIN_CAP = 1u << 21;
constexpr float LOG2E = 1.4426950408889634f;
constexpr float SCALE = 0.08838834764831845f;
constexpr u32 WS_EPOCH = 0, WS_FLAG = 4096, WS_QG = 8192, WS_R = WS_QG + 256 * 2048, WS_Y = WS_R + 256 * 640,
              WS_FP = WS_Y + 2 * 8 * 2560, WS_END = WS_FP + 108 * 2560, WS_DIAG = 2u << 20;
constexpr int AUX_NT = 2, AUX_SC1 = 16;
#define KAUX 2
#define VAUX 2
#define WAUX 2
constexpr int L_QP = 0;
constexpr int L_ML = 2048;
constexpr int L_QS = 2064;
constexpr int L_CNT = 2128;
constexpr int L_OS = 2132;
constexpr int L_ACT = 2644;
constexpr int L_END = 2692;

__device__ __forceinline__ rsrc_t mk(const void* p, u32 bytes) { return __builtin_amdgcn_make_buffer_rsrc((void*)p, 0, bytes, 0x00020000); }
template <int AUX> __device__ __forceinline__ u32x4 ld16(rsrc_t r, u32 v, u32 s) { return __builtin_amdgcn_raw_buffer_load_b128(r, v, s, AUX); }
template <int AUX> __device__ __forceinline__ u32x2 ld8(rsrc_t r, u32 v, u32 s) { return __builtin_amdgcn_raw_buffer_load_b64(r, v, s, AUX); }
__device__ __forceinline__ void st16(rsrc_t r, u32 v, u32x4 x) { __builtin_amdgcn_raw_buffer_store_b128(x, r, v, 0, AUX_SC1); }
__device__ __forceinline__ void st8(rsrc_t r, u32 v, u32x2 x) { __builtin_amdgcn_raw_buffer_store_b64(x, r, v, 0, AUX_SC1); }
__device__ __forceinline__ float asf(u32 x) { return __builtin_bit_cast(float, x); }
__device__ __forceinline__ u32 asu(float x) { return __builtin_bit_cast(u32, x); }
__device__ __forceinline__ float blo(u32 x) { return asf(x << 16); }
__device__ __forceinline__ float bhi(u32 x) { return asf(x & 0xffff0000u); }
__device__ __forceinline__ u32 pkbf(float a, float b) { bf16x2 p = {(__bf16)a, (__bf16)b}; return __builtin_bit_cast(u32, p); }
__device__ __forceinline__ float rbf(float x) { return (float)(__bf16)x; }
__device__ __forceinline__ float dot2(u32 a, u32 b, float c) { return __builtin_amdgcn_fdot2_f32_bf16(__builtin_bit_cast(bf16x2, a), __builtin_bit_cast(bf16x2, b), c, false); }
__device__ __forceinline__ f32x4 mfma(u32x4 a, u32x4 b, f32x4 c) { return __builtin_amdgcn_mfma_f32_16x16x32_bf16(__builtin_bit_cast(bf16x8, a), __builtin_bit_cast(bf16x8, b), c, 0, 0, 0); }
template <int C> __device__ __forceinline__ float dppf(float x) { return asf(__builtin_amdgcn_mov_dpp(asu(x), C, 0xf, 0xf, true)); }
template <int C> __device__ __forceinline__ u32 dppu(u32 x) { return __builtin_amdgcn_mov_dpp(x, C, 0xf, 0xf, true); }
constexpr int XOR1 = 0xB1, XOR2 = 0x4E, XOR7 = 0x141, ROR8 = 0x128, ROR4 = 0x124;
__device__ __forceinline__ float sum16(float x) { auto s = __builtin_amdgcn_permlane16_swap(asu(x), asu(x), false, false); return asf(s[0]) + asf(s[1]); }
__device__ __forceinline__ float sum32(float x) { auto s = __builtin_amdgcn_permlane32_swap(asu(x), asu(x), false, false); return asf(s[0]) + asf(s[1]); }
__device__ __forceinline__ float max16(float x) { auto s = __builtin_amdgcn_permlane16_swap(asu(x), asu(x), false, false); return fmaxf(asf(s[0]), asf(s[1])); }
__device__ __forceinline__ float max32(float x) { auto s = __builtin_amdgcn_permlane32_swap(asu(x), asu(x), false, false); return fmaxf(asf(s[0]), asf(s[1])); }
__device__ __forceinline__ float rowsum(float x) { x += dppf<XOR1>(x); x += dppf<XOR2>(x); x += dppf<XOR7>(x); x += dppf<ROR8>(x); return x; }
__device__ __forceinline__ u32 clean(float x) { u32 u = asu(x); return u == SENT ? 0x7FC00000u : u; }
__device__ __forceinline__ u32x4 clean4(f32x4 v) { return u32x4{clean(v[0]), clean(v[1]), clean(v[2]), clean(v[3])}; }
__device__ __forceinline__ bool full4(u32x4 v) { return (v[0] != SENT) & (v[1] != SENT) & (v[2] != SENT) & (v[3] != SENT); }
__device__ __forceinline__ f32x4 asf4(u32x4 v) { return __builtin_bit_cast(f32x4, v); }
__device__ __forceinline__ float ex2(float x) { return __builtin_amdgcn_exp2f(x); }
#define FENCE() asm volatile("" ::: "memory")
#define BAR() do { asm volatile("s_waitcnt lgkmcnt(0)" ::: "memory"); __builtin_amdgcn_s_barrier(); asm volatile("" ::: "memory"); } while (0)
#define SB() __builtin_amdgcn_sched_barrier(0)
#define STAMP(n) do { u64 t_; asm volatile("s_memrealtime %0\n\ts_waitcnt lgkmcnt(0)" : "=s"(t_) :: "memory"); stamp[n] = (u32)t_; } while (0)

__device__ __forceinline__ void transpose8(const u32x4 (&in)[8], u32x4 (&out)[8]) {
#pragma unroll
  for (int k = 0; k < 4; ++k)
#pragma unroll
    for (int d = 0; d < 4; ++d) {
      out[2 * d][k] = __builtin_amdgcn_perm(in[2 * k + 1][d], in[2 * k][d], 0x05040100u);
      out[2 * d + 1][k] = __builtin_amdgcn_perm(in[2 * k + 1][d], in[2 * k][d], 0x07060302u);
    }
#pragma unroll
  for (int e = 0; e < 8; ++e) asm volatile("" : "+v"(out[e]));
}

__global__ void __launch_bounds__(512, 2)
k_block(const void* __restrict__ px, const void* __restrict__ pk, const void* __restrict__ pv, const void* __restrict__ pwq,
        const void* __restrict__ pwo, const void* __restrict__ pwg, const void* __restrict__ pwu, const void* __restrict__ pwd,
        void* __restrict__ pout, void* __restrict__ pws) {
  __shared__ __attribute__((aligned(16))) u32 lds[L_END];
  float* const ldf = reinterpret_cast<float*>(lds);
  const int j = blockIdx.x, xcd = j & 7, ii = j >> 3;
  const int tid = threadIdx.x, lane = tid & 63, w = __builtin_amdgcn_readfirstlane(tid >> 6);
  const int r = lane >> 4, c = lane & 15;
  const rsrc_t rws = mk(pws, WS_END);
  u32 stamp[12] = {0, 0, 0, 0, 0, 0, 0, 0, 0, 0, 0, 0};
  STAMP(0);
  const int bh = (ii & 15) * 4 + (xcd >> 1), sp = (xcd & 1) + 2 * (ii >> 4);
  const int gn = ii - 10, gm = xcd + 8 * (gn >> 2);
  const bool isW = ii < 10, isG = gn >= 0 && gn < 16 && gm < 27, isF = ii >= 26 && ii < 31;
  const int gb = gn & 1, gc = 2 * gm + ((gn >> 1) & 1);
  const rsrc_t rx = mk(px, 2 * DM * 2);
  const u32 kvbase = (u32)(bh * SK + sp * 2048) * 256u;
  const u32 vo = lane * 16;
  u32x4 KV[NB][8];
  bool val[NB];
#define ISSUE(b, idx) do { const u32 nb_ = val[b] ? (u32)(2 * NH * SK * DH * 2) : 0u; const rsrc_t rk_ = mk(pk, nb_), rv_ = mk(pv, nb_); const u32 so_ = kvbase + (u32)(idx) * 4096u; \
    _Pragma("unroll") for (int t_ = 0; t_ < 4; ++t_) KV[b][t_] = ld16<KAUX>(rk_, vo + t_ * 1024, so_); \
    _Pragma("unroll") for (int t_ = 0; t_ < 4; ++t_) KV[b][4 + t_] = ld16<VAUX>(rv_, vo + t_ * 1024, so_); } while (0)
  u32 tag = 0, tag_hi = 0;
  if (w != 0) {
    __builtin_amdgcn_s_barrier();
#pragma unroll
    for (int b = 0; b < NB; ++b) { val[b] = true; ISSUE(b, b * 7 + w - 1); }
    FENCE(); SB();
    STAMP(1);
  } else {
    __builtin_amdgcn_s_setprio(3);
#pragma unroll
    for (int b = 0; b < NB; ++b) val[b] = false;
    const u32x4 s4 = {SENT, SENT, SENT, SENT};
    st16(rws, lane < 33 ? WS_R + (bh * 4 + sp) * 640 + vo : OOB, s4);
    {
      const u32 fa = WS_FP + (gb * 54 + gc) * 2560 + vo;
      st16(rws, isW ? (lane < 32 ? WS_Y + (r * 8 + xcd) * 2560 + ii * 256 + c * 16 : OOB) : (isG ? fa : OOB), s4);
      st16(rws, isG ? fa + 1024 : OOB, s4);
      st16(rws, isG && lane < 32 ? fa + 2048 : OOB, s4);
    }
    FENCE(); SB();
    const int qh = j >> 3, qk = j & 7;
    const u32x2 ep = ld8<AUX_SC1>(rws, WS_EPOCH, 0);
    u32x4 xc[3], xa, xb; u32x2 xd; u32x4 wqv[20];
    {
      const rsrc_t rwq = mk(pwq, DM * NH * DH * 2);
      const u32 hb = (lane >> 5) * 1280 + (lane & 31) * 16;
      xc[0] = ld16<0>(rx, hb, 0); xc[1] = ld16<0>(rx, hb + 512, 0); xc[2] = ld16<0>(rx, (lane & 31) < 16 ? hb + 1024 : OOB, 0);
      const u32 row0 = 80 * qk + 20 * r, xo = ((lane >> 3) & 1) * 1280 + row0 * 2;
      xa = ld16<0>(rx, xo, 0); xb = ld16<0>(rx, xo + 16, 0); xd = ld8<0>(rx, xo + 32, 0);
      const u32 wo_ = row0 * 8192 + qh * 256 + c * 16;
#pragma unroll
      for (int t = 0; t < 20; ++t) wqv[t] = ld16<WAUX>(rwq, wo_, t * 8192);
    }
    FENCE(); SB();
    __builtin_amdgcn_s_barrier();
    STAMP(1);
    if (lane == 0) lds[L_CNT] = 7 * NB;
    tag = (u32)__builtin_amdgcn_readfirstlane(ep[0]) + 1u;
    tag_hi = (u32)__builtin_amdgcn_readfirstlane(ep[1]) + (tag == 0u ? 1u : 0u);
    float s = 0.f;
#pragma unroll
    for (int k = 0; k < 3; ++k)
#pragma unroll
      for (int d = 0; d < 4; ++d) s += blo(xc[k][d]) + bhi(xc[k][d]);
    s = sum16(rowsum(s));
    const float mean = s * (1.f / DM);
    float v = 0.f;
#pragma unroll
    for (int k = 0; k < 3; ++k) {
      float vk = 0.f;
#pragma unroll
      for (int d = 0; d < 4; ++d) { const float a = blo(xc[k][d]) - mean, b2 = bhi(xc[k][d]) - mean; vk = fmaf(a, a, vk); vk = fmaf(b2, b2, vk); }
      v += (k < 2 || (lane & 31) < 16) ? vk : 0.f;
    }
    v = sum16(rowsum(v));
    const float mb = rbf(mean), rinv = 1.f / rbf(sqrtf(rbf(rbf(v * (1.f / DM)) + 1e-5f)));
    const float m0 = asf(__builtin_amdgcn_readlane(asu(mb), 0)), m1 = asf(__builtin_amdgcn_readlane(asu(mb), 32));
    const float i0 = asf(__builtin_amdgcn_readlane(asu(rinv), 0)), i1 = asf(__builtin_amdgcn_readlane(asu(rinv), 32));
    const bool b1 = (lane >> 3) & 1;
    const float mm = b1 ? m1 : m0, ri = b1 ? i1 : i0;
    const u32 xw[10] = {xa[0], xa[1], xa[2], xa[3], xb[0], xb[1], xb[2], xb[3], xd[0], xd[1]};
    u32 sl[4], sh[4];
#pragma unroll
    for (int k = 0; k < 4; ++k) {
      const bool on = ((lane & 7) >> 1) == k;
      sl[k] = on ? ((lane & 1) ? 0x01000c0cu : 0x0c0c0100u) : 0x0c0c0c0cu;
      sh[k] = on ? ((lane & 1) ? 0x03020c0cu : 0x0c0c0302u) : 0x0c0c0c0cu;
    }
    f32x4 qa = {0.f, 0.f, 0.f, 0.f}, qb2 = {0.f, 0.f, 0.f, 0.f};
#pragma unroll
    for (int t = 0; t < 10; ++t) {
      const u32 dp = pkbf(blo(xw[t]) - mm, bhi(xw[t]) - mm);
      const u32 xn = pkbf(blo(dp) * ri, bhi(dp) * ri);
      qa = mfma(u32x4{__builtin_amdgcn_perm(xn, xn, sl[0]), __builtin_amdgcn_perm(xn, xn, sl[1]), __builtin_amdgcn_perm(xn, xn, sl[2]), __builtin_amdgcn_perm(xn, xn, sl[3])}, wqv[2 * t], qa);
      qb2 = mfma(u32x4{__builtin_amdgcn_perm(xn, xn, sh[0]), __builtin_amdgcn_perm(xn, xn, sh[1]), __builtin_amdgcn_perm(xn, xn, sh[2]), __builtin_amdgcn_perm(xn, xn, sh[3])}, wqv[2 * t + 1], qb2);
    }
    qa += qb2;
    FENCE();
    st16(rws, WS_QG + j * 2048 + vo, u32x4{asu(qa[0]), tag, asu(qa[1]), tag});
    st16(rws, WS_QG + j * 2048 + 1024 + vo, u32x4{asu(qa[2]), tag, asu(qa[3]), tag});
    st8(rws, lane == 0 ? WS_FLAG + j * 8 : OOB, u32x2{tag, tag_hi});
    STAMP(2);
    const u32 qb = WS_QG + (u32)((bh & 31) * 8) * 2048u + (u32)(bh >> 5) * 512u;
    const u32 qo = (lane >> 5) * 1024 + (lane & 31) * 16;
    u32x4 g[8];
    for (u32 spins = 0; spins < SPIN_CAP; ++spins) {
      FENCE();
      bool ok = true;
#pragma unroll
      for (int k = 0; k < 8; ++k) g[k] = ld16<AUX_SC1>(rws, qo, qb + k * 2048);
#pragma unroll
      for (int k = 0; k < 8; ++k) ok &= (g[k][1] == tag) & (g[k][3] == tag);
      if (__all(ok)) break;
    }
    float q0 = 0.f, q1 = 0.f;
#pragma unroll
    for (int k = 0; k < 8; ++k) { q0 += asf(g[k][0]); q1 += asf(g[k][2]); }
    lds[L_QS + 4 * c + 2 * (r & 1) + (r >> 1)] = pkbf(q0, q1);
  }
  BAR();
  STAMP(3);
  const u32x4 qv = *reinterpret_cast<const u32x4*>(lds + L_QS + 4 * c);
  if (w == 0) {
    for (u32 spins = 0; spins < SPIN_CAP; ++spins) {
      FENCE();
      const u32x4 f0 = ld16<AUX_SC1>(rws, WS_FLAG + vo, 0), f1 = ld16<AUX_SC1>(rws, WS_FLAG + 1024 + vo, 0);
      const bool ok = (f0[0] == tag) & (f0[1] == tag_hi) & (f0[2] == tag) & (f0[3] == tag_hi) & (f1[0] == tag) & (f1[1] == tag_hi) & (f1[2] == tag) & (f1[3] == tag_hi);
      if (__all(ok)) break;
      __builtin_amdgcn_s_sleep(4);
    }
    __builtin_amdgcn_s_setprio(0);
  }
  u32 psel[4];
  {
    const u32 src = (lane & 8) ? 0x0302u : 0x0100u;
    const u32 one = (lane & 1) ? ((src << 16) | 0x0c0cu) : (0x0c0c0000u | src);
#pragma unroll
    for (int k = 0; k < 4; ++k) psel[k] = (((lane & 7) >> 1) == k) ? one : 0x0c0c0c0cu;
  }
  const void* pa = pwo; const void* pb = pwo; u32 na = 0, nb2 = 0, sa = 1280, va = 0, vb = 0;
  if (isW) { na = NH * DH * DM * 2; va = (512 * xcd + 64 * w + 8 * (lane >> 3)) * 1280 + ii * 128 + (lane & 7) * 16; }
  if (isG) {
    if (w < 5) { pa = pwg; pb = pwu; na = nb2 = DM * FF * 2; sa = 3456; va = vb = (128 * w + 8 * (lane >> 2)) * 3456 + gc * 64 + (lane & 3) * 16; }
    else { pa = pb = pwd; na = FF * DM * 2; nb2 = w < 7 ? na : 0u; va = (32 * gc + 8 * r) * 1280 + (w - 5) * 512 + c * 16; vb = va + 256; }
  }
  asm volatile("" : "+v"(va), "+v"(vb));
  float m = -INFINITY, l = 0.f;
  f32x4 acc = {0.f, 0.f, 0.f, 0.f};
#define COMPUTE(b) do { float d_[4]; \
    _Pragma("unroll") for (int t_ = 0; t_ < 4; ++t_) { float s_ = dot2(KV[b][t_][0], qv[0], 0.f); s_ = dot2(KV[b][t_][1], qv[1], s_); s_ = dot2(KV[b][t_][2], qv[2], s_); d_[t_] = dot2(KV[b][t_][3], qv[3], s_); } \
    const float a0_ = d_[0] + dppf<ROR8>(d_[0]), a2_ = d_[2] + dppf<ROR8>(d_[2]), a1_ = d_[1] + dppf<ROR8>(d_[1]), a3_ = d_[3] + dppf<ROR8>(d_[3]); \
    const float x0_ = (c & 8) ? a2_ : a0_, x1_ = (c & 8) ? a3_ : a1_; \
    const float y0_ = x0_ + dppf<XOR7>(x0_), y1_ = x1_ + dppf<XOR7>(x1_); \
    float z_ = (c & 4) ? y1_ : y0_; z_ += dppf<XOR2>(z_); z_ += dppf<XOR1>(z_); \
    const float sc_ = rbf(rbf(z_) * SCALE); \
    float mx_ = fmaxf(sc_, dppf<ROR4>(sc_)); mx_ = fmaxf(mx_, dppf<ROR8>(mx_)); mx_ = max32(max16(mx_)); \
    const float mn_ = fmaxf(m, mx_), al_ = ex2((m - mn_) * LOG2E), p_ = ex2((sc_ - mn_) * LOG2E); \
    m = mn_; l = fmaf(l, al_, p_); acc *= al_; \
    const u32 pk_ = pkbf(p_, p_ - rbf(p_)); \
    { const u32 bc_ = dppu<0x150>(pk_); acc = mfma(u32x4{__builtin_amdgcn_perm(bc_, bc_, psel[0]), __builtin_amdgcn_perm(bc_, bc_, psel[1]), __builtin_amdgcn_perm(bc_, bc_, psel[2]), __builtin_amdgcn_perm(bc_, bc_, psel[3])}, KV[b][4], acc); } \
    { const u32 bc_ = dppu<0x154>(pk_); acc = mfma(u32x4{__builtin_amdgcn_perm(bc_, bc_, psel[0]), __builtin_amdgcn_perm(bc_, bc_, psel[1]), __builtin_amdgcn_perm(bc_, bc_, psel[2]), __builtin_amdgcn_perm(bc_, bc_, psel[3])}, KV[b][5], acc); } \
    { const u32 bc_ = dppu<0x158>(pk_); acc = mfma(u32x4{__builtin_amdgcn_perm(bc_, bc_, psel[0]), __builtin_amdgcn_perm(bc_, bc_, psel[1]), __builtin_amdgcn_perm(bc_, bc_, psel[2]), __builtin_amdgcn_perm(bc_, bc_, psel[3])}, KV[b][6], acc); } \
    { const u32 bc_ = dppu<0x15C>(pk_); acc = mfma(u32x4{__builtin_amdgcn_perm(bc_, bc_, psel[0]), __builtin_amdgcn_perm(bc_, bc_, psel[1]), __builtin_amdgcn_perm(bc_, bc_, psel[2]), __builtin_amdgcn_perm(bc_, bc_, psel[3])}, KV[b][7], acc); } \
  } while (0)
  for (;;) {
#pragma unroll
    for (int b = 0; b < NB; ++b) {
      if (val[b]) COMPUTE(b);
      u32 nx = 0;
      if (lane == 0) nx = __hip_atomic_fetch_add(lds + L_CNT, 1u, __ATOMIC_RELAXED, __HIP_MEMORY_SCOPE_WORKGROUP);
      const u32 idx = (u32)__builtin_amdgcn_readfirstlane(nx);
      val[b] = idx < (u32)RPW;
      SB();
      ISSUE(b, idx);
      SB();
    }
    if (!val[NB - 1]) break;
  }
  STAMP(4);
  u32x4 WA[8], WB[8];
  {
    const rsrc_t ra = mk(pa, na), rb = mk(pb, nb2);
#pragma unroll
    for (int t = 0; t < 8; ++t) WA[t] = ld16<WAUX>(ra, va, t * sa);
#pragma unroll
    for (int t = 0; t < 8; ++t) WB[t] = ld16<WAUX>(rb, vb, t * sa);
  }
  SB();
#pragma unroll
  for (int b = 0; b < NB; ++b) if (val[b]) COMPUTE(b);
  STAMP(5);
  {
    float lt = l + dppf<ROR4>(l); lt += dppf<ROR8>(lt); lt = sum32(sum16(lt));
    const f32x4 a4 = {sum32(acc[0]), sum32(acc[1]), sum32(acc[2]), sum32(acc[3])};
    if (lane < 32) *reinterpret_cast<f32x4*>(ldf + L_QP + w * 128 + lane * 4) = a4;
    if (lane == 0) { ldf[L_ML + 2 * w] = m; ldf[L_ML + 2 * w + 1] = lt; }
  }
  BAR();
  STAMP(6);
  if (w == 0) {
    float mw[8], lw[8];
#pragma unroll
    for (int k = 0; k < 4; ++k) { const f32x4 t4 = *reinterpret_cast<const f32x4*>(ldf + L_ML + 4 * k); mw[2 * k] = t4[0]; lw[2 * k] = t4[1]; mw[2 * k + 1] = t4[2]; lw[2 * k + 1] = t4[3]; }
    float M = mw[0];
#pragma unroll
    for (int k = 1; k < 8; ++k) M = fmaxf(M, mw[k]);
    float L = 0.f, wt[8];
#pragma unroll
    for (int k = 0; k < 8; ++k) { wt[k] = ex2((mw[k] - M) * LOG2E); L = fmaf(wt[k], lw[k], L); }
    const int hf = lane >> 5, ll = lane & 31;
    f32x4 o4 = {0.f, 0.f, 0.f, 0.f};
#pragma unroll
    for (int k = 0; k < 4; ++k) o4 += (hf ? wt[4 + k] : wt[k]) * *reinterpret_cast<const f32x4*>(ldf + L_QP + (4 * hf + k) * 128 + ll * 4);
    o4 = f32x4{sum32(o4[0]), sum32(o4[1]), sum32(o4[2]), sum32(o4[3])};
    if (lane == 32) o4 = f32x4{M, L, 0.f, 0.f};
    const u32 ro = lane < 32 ? (u32)((ll & 15) * 32 + (ll >> 4) * 16) : (lane == 32 ? 512u : OOB);
    st16(rws, lane <= 32 ? WS_R + (bh * 4 + sp) * 640 + ro : OOB, clean4(o4));
  }
  STAMP(7);
  if (isW) {
    u32x4 T[8]; transpose8(WA, T);
    const u32 rb = WS_R + (u32)(((w >> 2) * 32 + 4 * xcd + (w & 3)) * 4) * 640u;
    const u32 va = (lane >> 5) * 640 + (lane & 31) * 16;
    u32x4 a0, a1; u32x2 ml;
    for (u32 spins = 0; spins < SPIN_CAP; ++spins) {
      FENCE();
      a0 = ld16<AUX_SC1>(rws, va, rb); a1 = ld16<AUX_SC1>(rws, va + 1280, rb); ml = ld8<AUX_SC1>(rws, (lane & 3) * 640 + 512, rb);
      if (__all(full4(a0) & full4(a1) & (ml[0] != SENT) & (ml[1] != SENT))) break;
    }
    STAMP(8);
    {
      const float ms = asf(ml[0]), ls = asf(ml[1]);
      float M = fmaxf(ms, dppf<XOR1>(ms)); M = fmaxf(M, dppf<XOR2>(M));
      const float ws_ = ex2((ms - M) * LOG2E);
      float L = ws_ * ls; L += dppf<XOR1>(L); L += dppf<XOR2>(L);
      const float wn = ws_ / L;
      const float w0 = dppf<0x00>(wn), w1 = dppf<0x55>(wn), w2 = dppf<0xAA>(wn), w3 = dppf<0xFF>(wn);
      const bool hf = lane >= 32;
      f32x4 o4 = (hf ? w1 : w0) * asf4(a0) + (hf ? w3 : w2) * asf4(a1);
      o4 = f32x4{sum32(o4[0]), sum32(o4[1]), sum32(o4[2]), sum32(o4[3])};
      if (lane < 32) *reinterpret_cast<u32x2*>(lds + L_OS + (w >> 2) * 256 + (w & 3) * 64 + lane * 2) = u32x2{pkbf(o4[0], o4[1]), pkbf(o4[2], o4[3])};
    }
    BAR();
    {
      const u32x4 af = *reinterpret_cast<const u32x4*>(lds + L_OS + (c >> 3) * 256 + 32 * w + 8 * r + 4 * ((c >> 2) & 1));
      f32x4 dd[8];
#pragma unroll
      for (int e = 0; e < 8; ++e) dd[e] = mfma(af, T[e], f32x4{0.f, 0.f, 0.f, 0.f});
      if ((r & 1) == (c >> 3)) {
        float* pp = ldf + L_QP + ((w * 2 + (r & 1)) * 2 + (r >> 1)) * 64 + 8 * (c & 7);
        *reinterpret_cast<f32x4*>(pp) = f32x4{dd[0][0], dd[1][0], dd[2][0], dd[3][0]};
        *reinterpret_cast<f32x4*>(pp + 4) = f32x4{dd[4][0], dd[5][0], dd[6][0], dd[7][0]};
      }
    }
    BAR();
    if (w == 0) {
      const int hf = lane >> 5;
      f32x4 y4 = {0.f, 0.f, 0.f, 0.f};
#pragma unroll
      for (int k = 0; k < 8; ++k) y4 += *reinterpret_cast<const f32x4*>(ldf + L_QP + (8 * hf + k) * 128 + (lane & 31) * 4);
      y4 = f32x4{sum32(y4[0]), sum32(y4[1]), sum32(y4[2]), sum32(y4[3])};
      st16(rws, lane < 32 ? WS_Y + ((r & 1) * 8 + xcd) * 2560 + ii * 256 + c * 16 : OOB, clean4(y4));
    }
    STAMP(9);
  } else if (isG) {
    u32x4 TA[8], TB[8]; transpose8(WA, TA); transpose8(WB, TB);
    if (w == 7) {
      u32x2 xq[3];
      xq[0] = ld8<0>(rx, gb * 1280 + lane * 8, 0); xq[1] = ld8<0>(rx, gb * 1280 + 512 + lane * 8, 0); xq[2] = ld8<0>(rx, lane < 32 ? gb * 1280 + 1024 + lane * 8 : OOB, 0);
      const u32 yb = WS_Y + gb * 8 * 2560;
      const u32 v2 = lane < 32 ? vo + 2048 : OOB;
      for (u32 spins = 0; spins < SPIN_CAP; ++spins) {
        FENCE();
        if (__all(full4(ld16<AUX_SC1>(rws, vo, yb)))) break;
        __builtin_amdgcn_s_sleep(1);
      }
      u32x4 y[8][3];
      for (u32 spins = 0; spins < SPIN_CAP; ++spins) {
        FENCE();
        bool ok = true;
#pragma unroll
        for (int g = 0; g < 8; ++g) { y[g][0] = ld16<AUX_SC1>(rws, vo, yb + g * 2560); y[g][1] = ld16<AUX_SC1>(rws, vo + 1024, yb + g * 2560); y[g][2] = ld16<AUX_SC1>(rws, v2, yb + g * 2560); }
#pragma unroll
        for (int g = 0; g < 8; ++g) ok &= full4(y[g][0]) & full4(y[g][1]) & full4(y[g][2]);
        if (__all(ok)) break;
      }
      STAMP(8);
      float h[12]; float s = 0.f;
#pragma unroll
      for (int k = 0; k < 3; ++k) {
        const f32x4 t4 = ((asf4(y[0][k]) + asf4(y[1][k])) + (asf4(y[2][k]) + asf4(y[3][k]))) + ((asf4(y[4][k]) + asf4(y[5][k])) + (asf4(y[6][k]) + asf4(y[7][k])));
        h[4 * k] = rbf(blo(xq[k][0]) + rbf(t4[0])); h[4 * k + 1] = rbf(bhi(xq[k][0]) + rbf(t4[1]));
        h[4 * k + 2] = rbf(blo(xq[k][1]) + rbf(t4[2])); h[4 * k + 3] = rbf(bhi(xq[k][1]) + rbf(t4[3]));
        s += (h[4 * k] + h[4 * k + 1]) + (h[4 * k + 2] + h[4 * k + 3]);
      }
      s = sum32(sum16(rowsum(s)));
      const float mean = s * (1.f / DM);
      float v = 0.f, v2s = 0.f;
#pragma unroll
      for (int k = 0; k < 8; ++k) { const float a = h[k] - mean; v = fmaf(a, a, v); }
#pragma unroll
      for (int k = 8; k < 12; ++k) { const float a = h[k] - mean; v2s = fmaf(a, a, v2s); }
      v += lane < 32 ? v2s : 0.f;
      v = sum32(sum16(rowsum(v)));
      const float mb = rbf(mean), rinv = 1.f / rbf(sqrtf(rbf(rbf(v * (1.f / DM)) + 1e-5f)));
#pragma unroll
      for (int k = 0; k < 3; ++k) {
        const u32x2 xn = {pkbf(rbf(h[4 * k] - mb) * rinv, rbf(h[4 * k + 1] - mb) * rinv), pkbf(rbf(h[4 * k + 2] - mb) * rinv, rbf(h[4 * k + 3] - mb) * rinv)};
        if (k < 2 || lane < 32) *reinterpret_cast<u32x2*>(lds + L_OS + k * 128 + lane * 2) = xn;
      }
    }
    BAR();
    if (w < 5) {
      const u32x4 af = *reinterpret_cast<const u32x4*>(lds + L_OS + 64 * w + 16 * r + 4 * (c >> 2));
      f32x4 da[8], db[8];
#pragma unroll
      for (int e = 0; e < 8; ++e) da[e] = mfma(af, TA[e], f32x4{0.f, 0.f, 0.f, 0.f});
#pragma unroll
      for (int e = 0; e < 8; ++e) db[e] = mfma(af, TB[e], f32x4{0.f, 0.f, 0.f, 0.f});
      if (r == (c >> 2)) {
        float* pp = ldf + L_QP + (w * 4 + r) * 64 + 8 * (c & 3);
        *reinterpret_cast<f32x4*>(pp) = f32x4{da[0][0], da[1][0], da[2][0], da[3][0]};
        *reinterpret_cast<f32x4*>(pp + 4) = f32x4{da[4][0], da[5][0], da[6][0], da[7][0]};
        *reinterpret_cast<f32x4*>(pp + 32) = f32x4{db[0][0], db[1][0], db[2][0], db[3][0]};
        *reinterpret_cast<f32x4*>(pp + 36) = f32x4{db[4][0], db[5][0], db[6][0], db[7][0]};
      }
    }
    BAR();
    if (w >= 5) {
      float s0 = 0.f, s1 = 0.f;
#pragma unroll
      for (int k = 0; k < 10; ++k) { s0 += ldf[L_QP + (2 * k) * 64 + lane]; s1 += ldf[L_QP + (2 * k + 1) * 64 + lane]; }
      const float gu = rbf(s0 + s1);
      const auto sw = __builtin_amdgcn_permlane32_swap(asu(gu), asu(gu), false, false);
      const float g = asf(sw[0]), u = asf(sw[1]);
      const float z = 0.7978845608028654f * fmaf(0.044715f * g * g, g, g);
      const float ge = rbf(g / (1.f + ex2(-2.f * LOG2E * z)));
      const float act = ge * u;
      const u32 ap = pkbf(act, dppf<XOR1>(act));
      u32* la = lds + L_ACT + (w - 5) * 16;
      if (lane < 32 && !(lane & 1)) la[lane >> 1] = ap;
      asm volatile("s_waitcnt lgkmcnt(0)" ::: "memory");
      const u32x4 af = *reinterpret_cast<const u32x4*>(la + 4 * r);
      f32x4 da[8], db[8];
#pragma unroll
      for (int e = 0; e < 8; ++e) da[e] = mfma(af, TA[e], f32x4{0.f, 0.f, 0.f, 0.f});
#pragma unroll
      for (int e = 0; e < 8; ++e) db[e] = mfma(af, TB[e], f32x4{0.f, 0.f, 0.f, 0.f});
      f32x4 o4;
#pragma unroll
      for (int v = 0; v < 4; ++v) { const float ea = (r & 1) ? da[4 + v][0] : da[v][0], eb = (r & 1) ? db[4 + v][0] : db[v][0]; o4[v] = (r >> 1) ? eb : ea; }
      const u32 fo = WS_FP + (gb * 54 + gc) * 2560 + (w - 5) * 1024 + (r >> 1) * 512 + c * 32 + (r & 1) * 16;
      st16(rws, (w < 7 || lane < 32) ? fo : OOB, clean4(o4));
    }
    STAMP(9);
  } else if (isF) {
    if (w == 0) {
      const int fu = (ii - 26) * 8 + xcd, fb = fu / 20, fc = fu - 20 * fb;
      const int ug = lane >> 3, q8 = lane & 7;
      const u32x2 xq = ld8<0>(rx, fb * 1280 + fc * 64 + q8 * 8, 0);
      u32x4 y;
      for (u32 spins = 0; spins < SPIN_CAP; ++spins) {
        FENCE();
        y = ld16<AUX_SC1>(rws, WS_Y + (fb * 8 + ug) * 2560 + fc * 128 + q8 * 16, 0);
        if (__all(full4(y))) break;
        __builtin_amdgcn_s_sleep(1);
      }
      f32x4 t4 = asf4(y);
#pragma unroll
      for (int v = 0; v < 4; ++v) t4[v] = sum32(sum16(t4[v] + dppf<ROR8>(t4[v])));
      const float h0 = rbf(blo(xq[0]) + rbf(t4[0])), h1 = rbf(bhi(xq[0]) + rbf(t4[1])), h2 = rbf(blo(xq[1]) + rbf(t4[2])), h3 = rbf(bhi(xq[1]) + rbf(t4[3]));
      STAMP(8);
      const u32 fa = WS_FP + (fb * 54 + ug) * 2560 + fc * 128 + q8 * 16;
      u32x4 p[7];
      for (u32 spins = 0; spins < SPIN_CAP; ++spins) {
        FENCE();
        bool ok = true;
#pragma unroll
        for (int k = 0; k < 7; ++k) p[k] = ld16<AUX_SC1>(rws, (k < 6 || ug < 6) ? fa : OOB, k * 8 * 2560);
#pragma unroll
        for (int k = 0; k < 7; ++k) ok &= full4(p[k]);
        if (__all(ok)) break;
      }
      f32x4 s4 = ((asf4(p[0]) + asf4(p[1])) + (asf4(p[2]) + asf4(p[3]))) + ((asf4(p[4]) + asf4(p[5])) + asf4(p[6]));
#pragma unroll
      for (int v = 0; v < 4; ++v) s4[v] = sum32(sum16(s4[v] + dppf<ROR8>(s4[v])));
      const rsrc_t ro = mk(pout, 2 * DM * 2);
      __builtin_amdgcn_raw_buffer_store_b64(u32x2{pkbf(h0 + rbf(s4[0]), h1 + rbf(s4[1])), pkbf(h2 + rbf(s4[2]), h3 + rbf(s4[3]))}, ro, lane < 8 ? (u32)(fb * 1280 + fc * 64 + q8 * 8) : OOB, 0, 0);
      if (fu == 0) st8(rws, lane == 0 ? WS_EPOCH : OOB, u32x2{tag, tag_hi});
      STAMP(9);
    }
  }
  if (j == 208 && w == 0) { STAMP(10); const u32 dl = stamp[LEAK] - stamp[0]; for (u32 it = 0; it < 100000u; ++it) { STAMP(11); if (stamp[11] - stamp[10] >= dl) break; } }
}
}

extern "C" void kernel_launch(void* const* d_in, const int* in_sizes, int n_in, void* d_out, int out_size, void* d_ws, size_t ws_size,
                              hipStream_t stream) {
  hipLaunchKernelGGL(blk::k_block, dim3(256), dim3(512), 0, stream, (const void*)d_in[0], (const void*)d_in[1], (const void*)d_in[2],
                     (const void*)d_in[3], (const void*)d_in[4], (const void*)d_in[6], (const void*)d_in[7], (const void*)d_in[8], d_out, d_ws);
}
```

```cpp
#include <hip/hip_runtime.h>
#include <stdint.h>

#define DIAG 1
#define LEAK2 1

namespace blk {
typedef unsigned u32;
typedef unsigned long long u64;
typedef u32 u32x2 __attribute__((ext_vector_type(2)));
typedef u32 u32x4 __attribute__((ext_vector_type(4)));
typedef float f32x4 __attribute__((ext_vector_type(4)));
typedef __bf16 bf16x2 __attribute__((ext_vector_type(2)));
typedef __bf16 bf16x8 __attribute__((ext_vector_type(8)));
typedef __amdgpu_buffer_rsrc_t rsrc_t;

constexpr int DM = 640, NH = 32, SK = 8192, DH = 128, FF = 1728;
constexpr int NB = 4;
constexpr int RPW = 128;
constexpr u32 SENT = 0xFFFFFFFFu;
constexpr u32 OOB = 0x7FFFFFF0u;
constexpr u32 SPIN_CAP = 1u << 21;
constexpr float LOG2E = 1.4426950408889634f;
constexpr float SCALE = 0.08838834764831845f;
constexpr u32 WS_EPOCH = 0, WS_FLAG = 4096, WS_QG = 8192, WS_R = WS_QG + 256 * 2048, WS_Y = WS_R + 256 * 640,
              WS_FP = WS_Y + 2 * 8 * 2560, WS_END = WS_FP + 108 * 2560, WS_DIAG = 2u << 20;
constexpr int AUX_NT = 2, AUX_SC1 = 16;
#define KAUX 2
#define VAUX 2
#define WAUX 2
constexpr int L_QP = 0;
constexpr int L_ML = 2048;
constexpr int L_QS = 2064;
constexpr int L_CNT = 2128;
constexpr int L_OS = 2132;
constexpr int L_ACT = 2644;
constexpr int L_END = 2692;

__device__ __forceinline__ rsrc_t mk(const void* p, u32 bytes) { return __builtin_amdgcn_make_buffer_rsrc((void*)p, 0, bytes, 0x00020000); }
template <int AUX> __device__ __forceinline__ u32x4 ld16(rsrc_t r, u32 v, u32 s) { return __builtin_amdgcn_raw_buffer_load_b128(r, v, s, AUX); }
template <int AUX> __device__ __forceinline__ u32x2 ld8(rsrc_t r, u32 v, u32 s) { return __builtin_amdgcn_raw_buffer_load_b64(r, v, s, AUX); }
__device__ __forceinline__ void st16(rsrc_t r, u32 v, u32x4 x) { __builtin_amdgcn_raw_buffer_store_b128(x, r, v, 0, AUX_SC1); }
__device__ __forceinline__ void st8(rsrc_t r, u32 v, u32x2 x) { __builtin_amdgcn_raw_buffer_store_b64(x, r, v, 0, AUX_SC1); }
__device__ __forceinline__ float asf(u32 x) { return __builtin_bit_cast(float, x); }
__device__ __forceinline__ u32 asu(float x) { return __builtin_bit_cast(u32, x); }
__device__ __forceinline__ float blo(u32 x) { return asf(x << 16); }
__device__ __forceinline__ float bhi(u32 x) { return asf(x & 0xffff0000u); }
__device__ __forceinline__ u32 pkbf(float a, float b) { bf16x2 p = {(__bf16)a, (__bf16)b}; return __builtin_bit_cast(u32, p); }
__device__ __forceinline__ float rbf(float x) { return (float)(__bf16)x; }
__device__ __forceinline__ float dot2(u32 a, u32 b, float c) { return __builtin_amdgcn_fdot2_f32_bf16(__builtin_bit_cast(bf16x2, a), __builtin_bit_cast(bf16x2, b), c, false); }
__device__ __forceinline__ f32x4 mfma(u32x4 a, u32x4 b, f32x4 c) { return __builtin_amdgcn_mfma_f32_16x16x32_bf16(__builtin_bit_cast(bf16x8, a), __builtin_bit_cast(bf16x8, b), c, 0, 0, 0); }
template <int C> __device__ __forceinline__ float dppf(float x) { return asf(__builtin_amdgcn_mov_dpp(asu(x), C, 0xf, 0xf, true)); }
template <int C> __device__ __forceinline__ u32 dppu(u32 x) { return __builtin_amdgcn_mov_dpp(x, C, 0xf, 0xf, true); }
constexpr int XOR1 = 0xB1, XOR2 = 0x4E, XOR7 = 0x141, ROR8 = 0x128, ROR4 = 0x124;
__device__ __forceinline__ float sum16(float x) { auto s = __builtin_amdgcn_permlane16_swap(asu(x), asu(x), false, false); return asf(s[0]) + asf(s[1]); }
__device__ __forceinline__ float sum32(float x) { auto s = __builtin_amdgcn_permlane32_swap(asu(x), asu(x), false, false); return asf(s[0]) + asf(s[1]); }
__device__ __forceinline__ float max16(float x) { auto s = __builtin_amdgcn_permlane16_swap(asu(x), asu(x), false, false); return fmaxf(asf(s[0]), asf(s[1])); }
__device__ __forceinline__ float max32(float x) { auto s = __builtin_amdgcn_permlane32_swap(asu(x), asu(x), false, false); return fmaxf(asf(s[0]), asf(s[1])); }
__device__ __forceinline__ float rowsum(float x) { x += dppf<XOR1>(x); x += dppf<XOR2>(x); x += dppf<XOR7>(x); x += dppf<ROR8>(x); return x; }
__device__ __forceinline__ u32 clean(float x) { u32 u = asu(x); return u == SENT ? 0x7FC00000u : u; }
__device__ __forceinline__ u32x4 clean4(f32x4 v) { return u32x4{clean(v[0]), clean(v[1]), clean(v[2]), clean(v[3])}; }
__device__ __forceinline__ bool full4(u32x4 v) { return (v[0] != SENT) & (v[1] != SENT) & (v[2] != SENT) & (v[3] != SENT); }
__device__ __forceinline__ f32x4 asf4(u32x4 v) { return __builtin_bit_cast(f32x4, v); }
__device__ __forceinline__ float ex2(float x) { return __builtin_amdgcn_exp2f(x); }
#define FENCE() asm volatile("" ::: "memory")
#define BAR() do { asm volatile("s_waitcnt lgkmcnt(0)" ::: "memory"); __builtin_amdgcn_s_barrier(); asm volatile("" ::: "memory"); } while (0)
#define SB() __builtin_amdgcn_sched_barrier(0)
#define STAMP(n) do { u64 t_; asm volatile("s_memrealtime %0\n\ts_waitcnt lgkmcnt(0)" : "=s"(t_) :: "memory"); stamp[n] = (u32)t_; } while (0)

__device__ __forceinline__ void transpose8(const u32x4 (&in)[8], u32x4 (&out)[8]) {
#pragma unroll
  for (int k = 0; k < 4; ++k)
#pragma unroll
    for (int d = 0; d < 4; ++d) {
      out[2 * d][k] = __builtin_amdgcn_perm(in[2 * k + 1][d], in[2 * k][d], 0x05040100u);
      out[2 * d + 1][k] = __builtin_amdgcn_perm(in[2 * k + 1][d], in[2 * k][d], 0x07060302u);
    }
#pragma unroll
  for (int e = 0; e < 8; ++e) asm volatile("" : "+v"(out[e]));
}

__global__ void __launch_bounds__(512, 2)
k_block(const void* __restrict__ px, const void* __restrict__ pk, const void* __restrict__ pv, const void* __restrict__ pwq,
        const void* __restrict__ pwo, const void* __restrict__ pwg, const void* __restrict__ pwu, const void* __restrict__ pwd,
        void* __restrict__ pout, void* __restrict__ pws) {
  __shared__ __attribute__((aligned(16))) u32 lds[L_END];
  float* const ldf = reinterpret_cast<float*>(lds);
  const int j = blockIdx.x, xcd = j & 7, ii = j >> 3;
  const int tid = threadIdx.x, lane = tid & 63, w = __builtin_amdgcn_readfirstlane(tid >> 6);
  const int r = lane >> 4, c = lane & 15;
  const rsrc_t rws = mk(pws, WS_END);
  u32 stamp[12] = {0, 0, 0, 0, 0, 0, 0, 0, 0, 0, 0, 0};
  STAMP(0);
  const int bh = (ii & 15) * 4 + (xcd >> 1), sp = (xcd & 1) + 2 * (ii >> 4);
  const int gn = ii - 10, gm = xcd + 8 * (gn >> 2);
  const bool isW = ii < 10, isG = gn >= 0 && gn < 16 && gm < 27, isF = ii >= 26 && ii < 31;
  const int gb = gn & 1, gc = 2 * gm + ((gn >> 1) & 1);
  const rsrc_t rx = mk(px, 2 * DM * 2);
  const u32 kvbase = (u32)(bh * SK + sp * 2048) * 256u;
  const u32 vo = lane * 16;
  u32x4 KV[NB][8];
  bool val[NB];
#define ISSUE(b, idx) do { const u32 nb_ = val[b] ? (u32)(2 * NH * SK * DH * 2) : 0u; const rsrc_t rk_ = mk(pk, nb_), rv_ = mk(pv, nb_); const u32 so_ = kvbase + (u32)(idx) * 4096u; \
    _Pragma("unroll") for (int t_ = 0; t_ < 4; ++t_) KV[b][t_] = ld16<KAUX>(rk_, vo + t_ * 1024, so_); \
    _Pragma("unroll") for (int t_ = 0; t_ < 4; ++t_) KV[b][4 + t_] = ld16<VAUX>(rv_, vo + t_ * 1024, so_); } while (0)
  u32 tag = 0, tag_hi = 0;
  if (w != 0) {
    __builtin_amdgcn_s_barrier();
#pragma unroll
    for (int b = 0; b < NB; ++b) { val[b] = true; ISSUE(b, b * 7 + w - 1); }
    FENCE(); SB();
    STAMP(1);
  } else {
    __builtin_amdgcn_s_setprio(3);
#pragma unroll
    for (int b = 0; b < NB; ++b) val[b] = false;
    const u32x4 s4 = {SENT, SENT, SENT, SENT};
    st16(rws, lane < 33 ? WS_R + (bh * 4 + sp) * 640 + vo : OOB, s4);
    {
      const u32 fa = WS_FP + (gb * 54 + gc) * 2560 + vo;
      st16(rws, isW ? (lane < 32 ? WS_Y + (r * 8 + xcd) * 2560 + ii * 256 + c * 16 : OOB) : (isG ? fa : OOB), s4);
      st16(rws, isG ? fa + 1024 : OOB, s4);
      st16(rws, isG && lane < 32 ? fa + 2048 : OOB, s4);
    }
    FENCE(); SB();
    const int qh = j >> 3, qk = j & 7;
    const u32x2 ep = ld8<AUX_SC1>(rws, WS_EPOCH, 0);
    u32x4 xc[3], xa, xb; u32x2 xd; u32x4 wqv[20];
    {
      const rsrc_t rwq = mk(pwq, DM * NH * DH * 2);
      const u32 hb = (lane >> 5) * 1280 + (lane & 31) * 16;
      xc[0] = ld16<0>(rx, hb, 0); xc[1] = ld16<0>(rx, hb + 512, 0); xc[2] = ld16<0>(rx, (lane & 31) < 16 ? hb + 1024 : OOB, 0);
      const u32 row0 = 80 * qk + 20 * r, xo = ((lane >> 3) & 1) * 1280 + row0 * 2;
      xa = ld16<0>(rx, xo, 0); xb = ld16<0>(rx, xo + 16, 0); xd = ld8<0>(rx, xo + 32, 0);
      const u32 wo_ = row0 * 8192 + qh * 256 + c * 16;
#pragma unroll
      for (int t = 0; t < 20; ++t) wqv[t] = ld16<WAUX>(rwq, wo_, t * 8192);
    }
    FENCE(); SB();
    __builtin_amdgcn_s_barrier();
    STAMP(1);
    if (lane == 0) lds[L_CNT] = 7 * NB;
    tag = (u32)__builtin_amdgcn_readfirstlane(ep[0]) + 1u;
    tag_hi = (u32)__builtin_amdgcn_readfirstlane(ep[1]) + (tag == 0u ? 1u : 0u);
    float s = 0.f;
#pragma unroll
    for (int k = 0; k < 3; ++k)
#pragma unroll
      for (int d = 0; d < 4; ++d) s += blo(xc[k][d]) + bhi(xc[k][d]);
    s = sum16(rowsum(s));
    const float mean = s * (1.f / DM);
    float v = 0.f;
#pragma unroll
    for (int k = 0; k < 3; ++k) {
      float vk = 0.f;
#pragma unroll
      for (int d = 0; d < 4; ++d) { const float a = blo(xc[k][d]) - mean, b2 = bhi(xc[k][d]) - mean; vk = fmaf(a, a, vk); vk = fmaf(b2, b2, vk); }
      v += (k < 2 || (lane & 31) < 16) ? vk : 0.f;
    }
    v = sum16(rowsum(v));
    const float mb = rbf(mean), rinv = 1.f / rbf(sqrtf(rbf(rbf(v * (1.f / DM)) + 1e-5f)));
    const float m0 = asf(__builtin_amdgcn_readlane(asu(mb), 0)), m1 = asf(__builtin_amdgcn_readlane(asu(mb), 32));
    const float i0 = asf(__builtin_amdgcn_readlane(asu(rinv), 0)), i1 = asf(__builtin_amdgcn_readlane(asu(rinv), 32));
    const bool b1 = (lane >> 3) & 1;
    const float mm = b1 ? m1 : m0, ri = b1 ? i1 : i0;
    const u32 xw[10] = {xa[0], xa[1], xa[2], xa[3], xb[0], xb[1], xb[2], xb[3], xd[0], xd[1]};
    u32 sl[4], sh[4];
#pragma unroll
    for (int k = 0; k < 4; ++k) {
      const bool on = ((lane & 7) >> 1) == k;
      sl[k] = on ? ((lane & 1) ? 0x01000c0cu : 0x0c0c0100u) : 0x0c0c0c0cu;
      sh[k] = on ? ((lane & 1) ? 0x03020c0cu : 0x0c0c0302u) : 0x0c0c0c0cu;
    }
    f32x4 qa = {0.f, 0.f, 0.f, 0.f}, qb2 = {0.f, 0.f, 0.f, 0.f};
#pragma unroll
    for (int t = 0; t < 10; ++t) {
      const u32 dp = pkbf(blo(xw[t]) - mm, bhi(xw[t]) - mm);
      const u32 xn = pkbf(blo(dp) * ri, bhi(dp) * ri);
      qa = mfma(u32x4{__builtin_amdgcn_perm(xn, xn, sl[0]), __builtin_amdgcn_perm(xn, xn, sl[1]), __builtin_amdgcn_perm(xn, xn, sl[2]), __builtin_amdgcn_perm(xn, xn, sl[3])}, wqv[2 * t], qa);
      qb2 = mfma(u32x4{__builtin_amdgcn_perm(xn, xn, sh[0]), __builtin_amdgcn_perm(xn, xn, sh[1]), __builtin_amdgcn_perm(xn, xn, sh[2]), __builtin_amdgcn_perm(xn, xn, sh[3])}, wqv[2 * t + 1], qb2);
    }
    qa += qb2;
    FENCE();
    st16(rws, WS_QG + j * 2048 + vo, u32x4{asu(qa[0]), tag, asu(qa[1]), tag});
    st16(rws, WS_QG + j * 2048 + 1024 + vo, u32x4{asu(qa[2]), tag, asu(qa[3]), tag});
    st8(rws, lane == 0 ? WS_FLAG + j * 8 : OOB, u32x2{tag, tag_hi});
    { const rsrc_t rd_ = mk((char*)pws + WS_DIAG, 8192); st8(rd_, lane == 0 ? (u32)(j * 8) : OOB, u32x2{stamp[0], tag}); }
    STAMP(2);
    const u32 qb = WS_QG + (u32)((bh & 31) * 8) * 2048u + (u32)(bh >> 5) * 512u;
    const u32 qo = (lane >> 5) * 1024 + (lane & 31) * 16;
    u32x4 g[8];
    for (u32 spins = 0; spins < SPIN_CAP; ++spins) {
      FENCE();
      bool ok = true;
#pragma unroll
      for (int k = 0; k < 8; ++k) g[k] = ld16<AUX_SC1>(rws, qo, qb + k * 2048);
#pragma unroll
      for (int k = 0; k < 8; ++k) ok &= (g[k][1] == tag) & (g[k][3] == tag);
      if (__all(ok)) break;
    }
    float q0 = 0.f, q1 = 0.f;
#pragma unroll
    for (int k = 0; k < 8; ++k) { q0 += asf(g[k][0]); q1 += asf(g[k][2]); }
    lds[L_QS + 4 * c + 2 * (r & 1) + (r >> 1)] = pkbf(q0, q1);
  }
  BAR();
  STAMP(3);
  const u32x4 qv = *reinterpret_cast<const u32x4*>(lds + L_QS + 4 * c);
  if (w == 0) {
    for (u32 spins = 0; spins < SPIN_CAP; ++spins) {
      FENCE();
      const u32x4 f0 = ld16<AUX_SC1>(rws, WS_FLAG + vo, 0), f1 = ld16<AUX_SC1>(rws, WS_FLAG + 1024 + vo, 0);
      const bool ok = (f0[0] == tag) & (f0[1] == tag_hi) & (f0[2] == tag) & (f0[3] == tag_hi) & (f1[0] == tag) & (f1[1] == tag_hi) & (f1[2] == tag) & (f1[3] == tag_hi);
      if (__all(ok)) break;
      __builtin_amdgcn_s_sleep(4);
    }
    __builtin_amdgcn_s_setprio(0);
  }
  u32 psel[4];
  {
    const u32 src = (lane & 8) ? 0x0302u : 0x0100u;
    const u32 one = (lane & 1) ? ((src << 16) | 0x0c0cu) : (0x0c0c0000u | src);
#pragma unroll
    for (int k = 0; k < 4; ++k) psel[k] = (((lane & 7) >> 1) == k) ? one : 0x0c0c0c0cu;
  }
  const void* pa = pwo; const void* pb = pwo; u32 na = 0, nb2 = 0, sa = 1280, va = 0, vb = 0;
  if (isW) { na = NH * DH * DM * 2; va = (512 * xcd + 64 * w + 8 * (lane >> 3)) * 1280 + ii * 128 + (lane & 7) * 16; }
  if (isG) {
    if (w < 5) { pa = pwg; pb = pwu; na = nb2 = DM * FF * 2; sa = 3456; va = vb = (128 * w + 8 * (lane >> 2)) * 3456 + gc * 64 + (lane & 3) * 16; }
    else { pa = pb = pwd; na = FF * DM * 2; nb2 = w < 7 ? na : 0u; va = (32 * gc + 8 * r) * 1280 + (w - 5) * 512 + c * 16; vb = va + 256; }
  }
  asm volatile("" : "+v"(va), "+v"(vb));
  float m = -INFINITY, l = 0.f;
  f32x4 acc = {0.f, 0.f, 0.f, 0.f};
#define COMPUTE(b) do { float d_[4]; \
    _Pragma("unroll") for (int t_ = 0; t_ < 4; ++t_) { float s_ = dot2(KV[b][t_][0], qv[0], 0.f); s_ = dot2(KV[b][t_][1], qv[1], s_); s_ = dot2(KV[b][t_][2], qv[2], s_); d_[t_] = dot2(KV[b][t_][3], qv[3], s_); } \
    const float a0_ = d_[0] + dppf<ROR8>(d_[0]), a2_ = d_[2] + dppf<ROR8>(d_[2]), a1_ = d_[1] + dppf<ROR8>(d_[1]), a3_ = d_[3] + dppf<ROR8>(d_[3]); \
    const float x0_ = (c & 8) ? a2_ : a0_, x1_ = (c & 8) ? a3_ : a1_; \
    const float y0_ = x0_ + dppf<XOR7>(x0_), y1_ = x1_ + dppf<XOR7>(x1_); \
    float z_ = (c & 4) ? y1_ : y0_; z_ += dppf<XOR2>(z_); z_ += dppf<XOR1>(z_); \
    const float sc_ = rbf(rbf(z_) * SCALE); \
    float mx_ = fmaxf(sc_, dppf<ROR4>(sc_)); mx_ = fmaxf(mx_, dppf<ROR8>(mx_)); mx_ = max32(max16(mx_)); \
    const float mn_ = fmaxf(m, mx_), al_ = ex2((m - mn_) * LOG2E), p_ = ex2((sc_ - mn_) * LOG2E); \
    m = mn_; l = fmaf(l, al_, p_); acc *= al_; \
    const u32 pk_ = pkbf(p_, p_ - rbf(p_)); \
    { const u32 bc_ = dppu<0x150>(pk_); acc = mfma(u32x4{__builtin_amdgcn_perm(bc_, bc_, psel[0]), __builtin_amdgcn_perm(bc_, bc_, psel[1]), __builtin_amdgcn_perm(bc_, bc_, psel[2]), __builtin_amdgcn_perm(bc_, bc_, psel[3])}, KV[b][4], acc); } \
    { const u32 bc_ = dppu<0x154>(pk_); acc = mfma(u32x4{__builtin_amdgcn_perm(bc_, bc_, psel[0]), __builtin_amdgcn_perm(bc_, bc_, psel[1]), __builtin_amdgcn_perm(bc_, bc_, psel[2]), __builtin_amdgcn_perm(bc_, bc_, psel[3])}, KV[b][5], acc); } \
    { const u32 bc_ = dppu<0x158>(pk_); acc = mfma(u32x4{__builtin_amdgcn_perm(bc_, bc_, psel[0]), __builtin_amdgcn_perm(bc_, bc_, psel[1]), __builtin_amdgcn_perm(bc_, bc_, psel[2]), __builtin_amdgcn_perm(bc_, bc_, psel[3])}, KV[b][6], acc); } \
    { const u32 bc_ = dppu<0x15C>(pk_); acc = mfma(u32x4{__builtin_amdgcn_perm(bc_, bc_, psel[0]), __builtin_amdgcn_perm(bc_, bc_, psel[1]), __builtin_amdgcn_perm(bc_, bc_, psel[2]), __builtin_amdgcn_perm(bc_, bc_, psel[3])}, KV[b][7], acc); } \
  } while (0)
  for (;;) {
#pragma unroll
    for (int b = 0; b < NB; ++b) {
      if (val[b]) COMPUTE(b);
      u32 nx = 0;
      if (lane == 0) nx = __hip_atomic_fetch_add(lds + L_CNT, 1u, __ATOMIC_RELAXED, __HIP_MEMORY_SCOPE_WORKGROUP);
      const u32 idx = (u32)__builtin_amdgcn_readfirstlane(nx);
      val[b] = idx < (u32)RPW;
      SB();
      ISSUE(b, idx);
      SB();
    }
    if (!val[NB - 1]) break;
  }
  STAMP(4);
  u32x4 WA[8], WB[8];
  {
    const rsrc_t ra = mk(pa, na), rb = mk(pb, nb2);
#pragma unroll
    for (int t = 0; t < 8; ++t) WA[t] = ld16<WAUX>(ra, va, t * sa);
#pragma unroll
    for (int t = 0; t < 8; ++t) WB[t] = ld16<WAUX>(rb, vb, t * sa);
  }
  SB();
#pragma unroll
  for (int b = 0; b < NB; ++b) if (val[b]) COMPUTE(b);
  STAMP(5);
  {
    float lt = l + dppf<ROR4>(l); lt += dppf<ROR8>(lt); lt = sum32(sum16(lt));
    const f32x4 a4 = {sum32(acc[0]), sum32(acc[1]), sum32(acc[2]), sum32(acc[3])};
    if (lane < 32) *reinterpret_cast<f32x4*>(ldf + L_QP + w * 128 + lane * 4) = a4;
    if (lane == 0) { ldf[L_ML + 2 * w] = m; ldf[L_ML + 2 * w + 1] = lt; }
  }
  BAR();
  STAMP(6);
  if (w == 0) {
    float mw[8], lw[8];
#pragma unroll
    for (int k = 0; k < 4; ++k) { const f32x4 t4 = *reinterpret_cast<const f32x4*>(ldf + L_ML + 4 * k); mw[2 * k] = t4[0]; lw[2 * k] = t4[1]; mw[2 * k + 1] = t4[2]; lw[2 * k + 1] = t4[3]; }
    float M = mw[0];
#pragma unroll
    for (int k = 1; k < 8; ++k) M = fmaxf(M, mw[k]);
    float L = 0.f, wt[8];
#pragma unroll
    for (int k = 0; k < 8; ++k) { wt[k] = ex2((mw[k] - M) * LOG2E); L = fmaf(wt[k], lw[k], L); }
    const int hf = lane >> 5, ll = lane & 31;
    f32x4 o4 = {0.f, 0.f, 0.f, 0.f};
#pragma unroll
    for (int k = 0; k < 4; ++k) o4 += (hf ? wt[4 + k] : wt[k]) * *reinterpret_cast<const f32x4*>(ldf + L_QP + (4 * hf + k) * 128 + ll * 4);
    o4 = f32x4{sum32(o4[0]), sum32(o4[1]), sum32(o4[2]), sum32(o4[3])};
    if (lane == 32) o4 = f32x4{M, L, 0.f, 0.f};
    const u32 ro = lane < 32 ? (u32)((ll & 15) * 32 + (ll >> 4) * 16) : (lane == 32 ? 512u : OOB);
    st16(rws, lane <= 32 ? WS_R + (bh * 4 + sp) * 640 + ro : OOB, clean4(o4));
  }
  STAMP(7);
  if (isW) {
    u32x4 T[8]; transpose8(WA, T);
    const u32 rb = WS_R + (u32)(((w >> 2) * 32 + 4 * xcd + (w & 3)) * 4) * 640u;
    const u32 va = (lane >> 5) * 640 + (lane & 31) * 16;
    u32x4 a0, a1; u32x2 ml;
    for (u32 spins = 0; spins < SPIN_CAP; ++spins) {
      FENCE();
      a0 = ld16<AUX_SC1>(rws, va, rb); a1 = ld16<AUX_SC1>(rws, va + 1280, rb); ml = ld8<AUX_SC1>(rws, (lane & 3) * 640 + 512, rb);
      if (__all(full4(a0) & full4(a1) & (ml[0] != SENT) & (ml[1] != SENT))) break;
    }
    STAMP(8);
    {
      const float ms = asf(ml[0]), ls = asf(ml[1]);
      float M = fmaxf(ms, dppf<XOR1>(ms)); M = fmaxf(M, dppf<XOR2>(M));
      const float ws_ = ex2((ms - M) * LOG2E);
      float L = ws_ * ls; L += dppf<XOR1>(L); L += dppf<XOR2>(L);
      const float wn = ws_ / L;
      const float w0 = dppf<0x00>(wn), w1 = dppf<0x55>(wn), w2 = dppf<0xAA>(wn), w3 = dppf<0xFF>(wn);
      const bool hf = lane >= 32;
      f32x4 o4 = (hf ? w1 : w0) * asf4(a0) + (hf ? w3 : w2) * asf4(a1);
      o4 = f32x4{sum32(o4[0]), sum32(o4[1]), sum32(o4[2]), sum32(o4[3])};
      if (lane < 32) *reinterpret_cast<u32x2*>(lds + L_OS + (w >> 2) * 256 + (w & 3) * 64 + lane * 2) = u32x2{pkbf(o4[0], o4[1]), pkbf(o4[2], o4[3])};
    }
    BAR();
    {
      const u32x4 af = *reinterpret_cast<const u32x4*>(lds + L_OS + (c >> 3) * 256 + 32 * w + 8 * r + 4 * ((c >> 2) & 1));
      f32x4 dd[8];
#pragma unroll
      for (int e = 0; e < 8; ++e) dd[e] = mfma(af, T[e], f32x4{0.f, 0.f, 0.f, 0.f});
      if ((r & 1) == (c >> 3)) {
        float* pp = ldf + L_QP + ((w * 2 + (r & 1)) * 2 + (r >> 1)) * 64 + 8 * (c & 7);
        *reinterpret_cast<f32x4*>(pp) = f32x4{dd[0][0], dd[1][0], dd[2][0], dd[3][0]};
        *reinterpret_cast<f32x4*>(pp + 4) = f32x4{dd[4][0], dd[5][0], dd[6][0], dd[7][0]};
      }
    }
    BAR();
    if (w == 0) {
      const int hf = lane >> 5;
      f32x4 y4 = {0.f, 0.f, 0.f, 0.f};
#pragma unroll
      for (int k = 0; k < 8; ++k) y4 += *reinterpret_cast<const f32x4*>(ldf + L_QP + (8 * hf + k) * 128 + (lane & 31) * 4);
      y4 = f32x4{sum32(y4[0]), sum32(y4[1]), sum32(y4[2]), sum32(y4[3])};
      st16(rws, lane < 32 ? WS_Y + ((r & 1) * 8 + xcd) * 2560 + ii * 256 + c * 16 : OOB, clean4(y4));
    }
    STAMP(9);
  } else if (isG) {
    u32x4 TA[8], TB[8]; transpose8(WA, TA); transpose8(WB, TB);
    if (w == 7) {
      u32x2 xq[3];
      xq[0] = ld8<0>(rx, gb * 1280 + lane * 8, 0); xq[1] = ld8<0>(rx, gb * 1280 + 512 + lane * 8, 0); xq[2] = ld8<0>(rx, lane < 32 ? gb * 1280 + 1024 + lane * 8 : OOB, 0);
      const u32 yb = WS_Y + gb * 8 * 2560;
      const u32 v2 = lane < 32 ? vo + 2048 : OOB;
      for (u32 spins = 0; spins < SPIN_CAP; ++spins) {
        FENCE();
        if (__all(full4(ld16<AUX_SC1>(rws, vo, yb)))) break;
        __builtin_amdgcn_s_sleep(1);
      }
      u32x4 y[8][3];
      for (u32 spins = 0; spins < SPIN_CAP; ++spins) {
        FENCE();
        bool ok = true;
#pragma unroll
        for (int g = 0; g < 8; ++g) { y[g][0] = ld16<AUX_SC1>(rws, vo, yb + g * 2560); y[g][1] = ld16<AUX_SC1>(rws, vo + 1024, yb + g * 2560); y[g][2] = ld16<AUX_SC1>(rws, v2, yb + g * 2560); }
#pragma unroll
        for (int g = 0; g < 8; ++g) ok &= full4(y[g][0]) & full4(y[g][1]) & full4(y[g][2]);
        if (__all(ok)) break;
      }
      STAMP(8);
      float h[12]; float s = 0.f;
#pragma unroll
      for (int k = 0; k < 3; ++k) {
        const f32x4 t4 = ((asf4(y[0][k]) + asf4(y[1][k])) + (asf4(y[2][k]) + asf4(y[3][k]))) + ((asf4(y[4][k]) + asf4(y[5][k])) + (asf4(y[6][k]) + asf4(y[7][k])));
        h[4 * k] = rbf(blo(xq[k][0]) + rbf(t4[0])); h[4 * k + 1] = rbf(bhi(xq[k][0]) + rbf(t4[1]));
        h[4 * k + 2] = rbf(blo(xq[k][1]) + rbf(t4[2])); h[4 * k + 3] = rbf(bhi(xq[k][1]) + rbf(t4[3]));
        s += (h[4 * k] + h[4 * k + 1]) + (h[4 * k + 2] + h[4 * k + 3]);
      }
      s = sum32(sum16(rowsum(s)));
      const float mean = s * (1.f / DM);
      float v = 0.f, v2s = 0.f;
#pragma unroll
      for (int k = 0; k < 8; ++k) { const float a = h[k] - mean; v = fmaf(a, a, v); }
#pragma unroll
      for (int k = 8; k < 12; ++k) { const float a = h[k] - mean; v2s = fmaf(a, a, v2s); }
      v += lane < 32 ? v2s : 0.f;
      v = sum32(sum16(rowsum(v)));
      const float mb = rbf(mean), rinv = 1.f / rbf(sqrtf(rbf(rbf(v * (1.f / DM)) + 1e-5f)));
#pragma unroll
      for (int k = 0; k < 3; ++k) {
        const u32x2 xn = {pkbf(rbf(h[4 * k] - mb) * rinv, rbf(h[4 * k + 1] - mb) * rinv), pkbf(rbf(h[4 * k + 2] - mb) * rinv, rbf(h[4 * k + 3] - mb) * rinv)};
        if (k < 2 || lane < 32) *reinterpret_cast<u32x2*>(lds + L_OS + k * 128 + lane * 2) = xn;
      }
    }
    BAR();
    if (w < 5) {
      const u32x4 af = *reinterpret_cast<const u32x4*>(lds + L_OS + 64 * w + 16 * r + 4 * (c >> 2));
      f32x4 da[8], db[8];
#pragma unroll
      for (int e = 0; e < 8; ++e) da[e] = mfma(af, TA[e], f32x4{0.f, 0.f, 0.f, 0.f});
#pragma unroll
      for (int e = 0; e < 8; ++e) db[e] = mfma(af, TB[e], f32x4{0.f, 0.f, 0.f, 0.f});
      if (r == (c >> 2)) {
        float* pp = ldf + L_QP + (w * 4 + r) * 64 + 8 * (c & 3);
        *reinterpret_cast<f32x4*>(pp) = f32x4{da[0][0], da[1][0], da[2][0], da[3][0]};
        *reinterpret_cast<f32x4*>(pp + 4) = f32x4{da[4][0], da[5][0], da[6][0], da[7][0]};
        *reinterpret_cast<f32x4*>(pp + 32) = f32x4{db[0][0], db[1][0], db[2][0], db[3][0]};
        *reinterpret_cast<f32x4*>(pp + 36) = f32x4{db[4][0], db[5][0], db[6][0], db[7][0]};
      }
    }
    BAR();
    if (w >= 5) {
      float s0 = 0.f, s1 = 0.f;
#pragma unroll
      for (int k = 0; k < 10; ++k) { s0 += ldf[L_QP + (2 * k) * 64 + lane]; s1 += ldf[L_QP + (2 * k + 1) * 64 + lane]; }
      const float gu = rbf(s0 + s1);
      const auto sw = __builtin_amdgcn_permlane32_swap(asu(gu), asu(gu), false, false);
      const float g = asf(sw[0]), u = asf(sw[1]);
      const float z = 0.7978845608028654f * fmaf(0.044715f * g * g, g, g);
      const float ge = rbf(g / (1.f + ex2(-2.f * LOG2E * z)));
      const float act = ge * u;
      const u32 ap = pkbf(act, dppf<XOR1>(act));
      u32* la = lds + L_ACT + (w - 5) * 16;
      if (lane < 32 && !(lane & 1)) la[lane >> 1] = ap;
      asm volatile("s_waitcnt lgkmcnt(0)" ::: "memory");
      const u32x4 af = *reinterpret_cast<const u32x4*>(la + 4 * r);
      f32x4 da[8], db[8];
#pragma unroll
      for (int e = 0; e < 8; ++e) da[e] = mfma(af, TA[e], f32x4{0.f, 0.f, 0.f, 0.f});
#pragma unroll
      for (int e = 0; e < 8; ++e) db[e] = mfma(af, TB[e], f32x4{0.f, 0.f, 0.f, 0.f});
      f32x4 o4;
#pragma unroll
      for (int v = 0; v < 4; ++v) { const float ea = (r & 1) ? da[4 + v][0] : da[v][0], eb = (r & 1) ? db[4 + v][0] : db[v][0]; o4[v] = (r >> 1) ? eb : ea; }
      const u32 fo = WS_FP + (gb * 54 + gc) * 2560 + (w - 5) * 1024 + (r >> 1) * 512 + c * 32 + (r & 1) * 16;
      st16(rws, (w < 7 || lane < 32) ? fo : OOB, clean4(o4));
    }
    STAMP(9);
  } else if (isF) {
    if (w == 0) {
      const int fu = (ii - 26) * 8 + xcd, fb = fu / 20, fc = fu - 20 * fb;
      const int ug = lane >> 3, q8 = lane & 7;
      const u32x2 xq = ld8<0>(rx, fb * 1280 + fc * 64 + q8 * 8, 0);
      u32x4 y;
      for (u32 spins = 0; spins < SPIN_CAP; ++spins) {
        FENCE();
        y = ld16<AUX_SC1>(rws, WS_Y + (fb * 8 + ug) * 2560 + fc * 128 + q8 * 16, 0);
        if (__all(full4(y))) break;
        __builtin_amdgcn_s_sleep(1);
      }
      f32x4 t4 = asf4(y);
#pragma unroll
      for (int v = 0; v < 4; ++v) t4[v] = sum32(sum16(t4[v] + dppf<ROR8>(t4[v])));
      const float h0 = rbf(blo(xq[0]) + rbf(t4[0])), h1 = rbf(bhi(xq[0]) + rbf(t4[1])), h2 = rbf(blo(xq[1]) + rbf(t4[2])), h3 = rbf(bhi(xq[1]) + rbf(t4[3]));
      STAMP(8);
      const u32 fa = WS_FP + (fb * 54 + ug) * 2560 + fc * 128 + q8 * 16;
      u32x4 p[7];
      for (u32 spins = 0; spins < SPIN_CAP; ++spins) {
        FENCE();
        bool ok = true;
#pragma unroll
        for (int k = 0; k < 7; ++k) p[k] = ld16<AUX_SC1>(rws, (k < 6 || ug < 6) ? fa : OOB, k * 8 * 2560);
#pragma unroll
        for (int k = 0; k < 7; ++k) ok &= full4(p[k]);
        if (__all(ok)) break;
      }
      f32x4 s4 = ((asf4(p[0]) + asf4(p[1])) + (asf4(p[2]) + asf4(p[3]))) + ((asf4(p[4]) + asf4(p[5])) + asf4(p[6]));
#pragma unroll
      for (int v = 0; v < 4; ++v) s4[v] = sum32(sum16(s4[v] + dppf<ROR8>(s4[v])));
      const rsrc_t ro = mk(pout, 2 * DM * 2);
      __builtin_amdgcn_raw_buffer_store_b64(u32x2{pkbf(h0 + rbf(s4[0]), h1 + rbf(s4[1])), pkbf(h2 + rbf(s4[2]), h3 + rbf(s4[3]))}, ro, lane < 8 ? (u32)(fb * 1280 + fc * 64 + q8 * 8) : OOB, 0, 0);
      if (fu == 0) st8(rws, lane == 0 ? WS_EPOCH : OOB, u32x2{tag, tag_hi});
      STAMP(9);
    }
  }
  if (j == 208 && w == 0) {
    const rsrc_t rd_ = mk((char*)pws + WS_DIAG, 8192);
    const u32x4 a_ = ld16<AUX_SC1>(rd_, vo, 0), b_ = ld16<AUX_SC1>(rd_, vo + 1024, 0);
    const u32x2 pe_ = ld8<AUX_SC1>(rd_, 4096, 0);
    int d0 = (int)(a_[0] - stamp[0]), d1 = (int)(a_[2] - stamp[0]), d2 = (int)(b_[0] - stamp[0]), d3 = (int)(b_[2] - stamp[0]);
    int mn = min(min(d0, d1), min(d2, d3)), mxv = max(max(d0, d1), max(d2, d3));
    for (int o = 1; o < 64; o <<= 1) { mn = min(mn, __shfl_xor(mn, o)); mxv = max(mxv, __shfl_xor(mxv, o)); }
    mn = __builtin_amdgcn_readfirstlane(mn); mxv = __builtin_amdgcn_readfirstlane(mxv);
    int gap = (int)(stamp[0] + (u32)mn - (u32)__builtin_amdgcn_readfirstlane(pe_[0]));
    if ((u32)__builtin_amdgcn_readfirstlane(pe_[1]) != tag - 1u || gap < 0 || gap > 5000) gap = 0;
    u32 dl = LEAK2 == 0 ? 0u : LEAK2 == 1 ? (u32)(-mn) * 4u : LEAK2 == 2 ? (u32)(mxv - mn) * 4u : (u32)gap;
    if (dl > 10000u) dl = 10000u;
    STAMP(10);
    for (u32 it = 0; it < 100000u; ++it) { STAMP(11); if (stamp[11] - stamp[10] >= dl) break; }
    STAMP(10);
    st8(rd_, lane == 0 ? 4096u : OOB, u32x2{stamp[10], tag});
  }
}
}

extern "C" void kernel_launch(void* const* d_in, const int* in_sizes, int n_in, void* d_out, int out_size, void* d_ws, size_t ws_size,
                              hipStream_t stream) {
  hipLaunchKernelGGL(blk::k_block, dim3(256), dim3(512), 0, stream, (const void*)d_in[0], (const void*)d_in[1], (const void*)d_in[2],
                     (const void*)d_in[3], (const void*)d_in[4], (const void*)d_in[6], (const void*)d_in[7], (const void*)d_in[8], d_out, d_ws);
}
```

```cpp
#include <hip/hip_runtime.h>
#include <stdint.h>

namespace blk {
typedef unsigned u32;
typedef unsigned long long u64;
typedef u32 u32x2 __attribute__((ext_vector_type(2)));
typedef u32 u32x4 __attribute__((ext_vector_type(4)));
typedef float f32x2 __attribute__((ext_vector_type(2)));
typedef float f32x4 __attribute__((ext_vector_type(4)));
typedef __bf16 bf16x2 __attribute__((ext_vector_type(2)));
typedef __bf16 bf16x8 __attribute__((ext_vector_type(8)));
typedef __amdgpu_buffer_rsrc_t rsrc_t;
typedef __attribute__((address_space(3))) u32 lds_u32;

constexpr int DM = 640, NH = 32, SK = 8192, DH = 128, FF = 1728;
constexpr u32 SENT = 0xFFFFFFFFu;
constexpr u32 OOB = 0x7FFFFFF0u;
constexpr u32 SPIN_CAP = 1u << 21;
constexpr float LOG2E = 1.4426950408889634f;
constexpr int AUX_NT = 2, AUX_SC1 = 16;
constexpr int FW = 3;
constexpr int QW = 4;
constexpr u32 WS_EPOCH = 0, WS_FLAG = 4096, WS_QG = 8192, WS_R = WS_QG + 256 * 2048, WS_Y = WS_R + 256 * 640,
              WS_FP = WS_Y + 2 * 8 * 2560, WS_END = WS_FP + 108 * 2560;
constexpr int L_MG = 0;
constexpr int L_CS = 2048;
constexpr int L_ML = 3328;
constexpr int L_QS = 3344;
constexpr int L_CNT = 3408;
constexpr int L_ST = 3412;
constexpr int L_TAG = 3424;
constexpr int L_PF = 3428;
constexpr int L_END = 3492;

__device__ __forceinline__ rsrc_t mk(const void* p, u32 bytes) { return __builtin_amdgcn_make_buffer_rsrc((void*)p, 0, bytes, 0x00020000); }
__device__ __forceinline__ u32x4 desc(const void* p, u32 bytes) { const u64 a = (u64)p; return u32x4{(u32)a, (u32)(a >> 32) & 0xffffu, bytes, 0x00020000u}; }
template <int AUX> __device__ __forceinline__ u32x4 ld16(rsrc_t r, u32 v, u32 s) { return __builtin_amdgcn_raw_buffer_load_b128(r, v, s, AUX); }
template <int AUX> __device__ __forceinline__ u32x2 ld8(rsrc_t r, u32 v, u32 s) { return __builtin_amdgcn_raw_buffer_load_b64(r, v, s, AUX); }
__device__ __forceinline__ void st16(rsrc_t r, u32 v, u32x4 x) { __builtin_amdgcn_raw_buffer_store_b128(x, r, v, 0, AUX_SC1); }
__device__ __forceinline__ void st8(rsrc_t r, u32 v, u32x2 x) { __builtin_amdgcn_raw_buffer_store_b64(x, r, v, 0, AUX_SC1); }
__device__ __forceinline__ float asf(u32 x) { return __builtin_bit_cast(float, x); }
__device__ __forceinline__ u32 asu(float x) { return __builtin_bit_cast(u32, x); }
__device__ __forceinline__ f32x2 asf2(u32x2 v) { return __builtin_bit_cast(f32x2, v); }
__device__ __forceinline__ f32x4 asf4(u32x4 v) { return __builtin_bit_cast(f32x4, v); }
__device__ __forceinline__ float blo(u32 x) { return asf(x << 16); }
__device__ __forceinline__ float bhi(u32 x) { return asf(x & 0xffff0000u); }
__device__ __forceinline__ u32 pkbf(float a, float b) { bf16x2 p = {(__bf16)a, (__bf16)b}; return __builtin_bit_cast(u32, p); }
__device__ __forceinline__ f32x4 mfma(u32x4 a, u32x4 b, f32x4 c) { return __builtin_amdgcn_mfma_f32_16x16x32_bf16(__builtin_bit_cast(bf16x8, a), __builtin_bit_cast(bf16x8, b), c, 0, 0, 0); }
template <int C> __device__ __forceinline__ float dppf(float x) { return asf(__builtin_amdgcn_mov_dpp(asu(x), C, 0xf, 0xf, true)); }
template <int C> __device__ __forceinline__ u32 dppu(u32 x) { return __builtin_amdgcn_mov_dpp(x, C, 0xf, 0xf, true); }
constexpr int XOR1 = 0xB1, XOR2 = 0x4E, XOR7 = 0x141, ROR8 = 0x128;
__device__ __forceinline__ float sum16(float x) { auto s = __builtin_amdgcn_permlane16_swap(asu(x), asu(x), false, false); return asf(s[0]) + asf(s[1]); }
__device__ __forceinline__ float sum32(float x) { auto s = __builtin_amdgcn_permlane32_swap(asu(x), asu(x), false, false); return asf(s[0]) + asf(s[1]); }
__device__ __forceinline__ float rowsum(float x) { x += dppf<XOR1>(x); x += dppf<XOR2>(x); x += dppf<XOR7>(x); x += dppf<ROR8>(x); return x; }
__device__ __forceinline__ u32 clean(float x) { const u32 u = asu(x); return u < SENT - 1u ? u : SENT - 1u; }
__device__ __forceinline__ u32x4 clean4(f32x4 v) { return u32x4{clean(v[0]), clean(v[1]), clean(v[2]), clean(v[3])}; }
__device__ __forceinline__ u32 umax(u32 a, u32 b) { return a > b ? a : b; }
__device__ __forceinline__ u32 max4(u32x4 v) { return umax(umax(v[0], v[1]), umax(v[2], v[3])); }
__device__ __forceinline__ u32x4 quad4(u32 x) { return u32x4{dppu<0x00>(x), dppu<0x55>(x), dppu<0xAA>(x), dppu<0xFF>(x)}; }
__device__ __forceinline__ u32x4 perm4(u32 x, const u32 (&s)[4]) {
  return u32x4{__builtin_amdgcn_perm(x, x, s[0]), __builtin_amdgcn_perm(x, x, s[1]), __builtin_amdgcn_perm(x, x, s[2]), __builtin_amdgcn_perm(x, x, s[3])};
}
__device__ __forceinline__ float red8(f32x4 v, int lane) {
  const float a0 = v[0] + dppf<ROR8>(v[0]), a1 = v[1] + dppf<ROR8>(v[1]), a2 = v[2] + dppf<ROR8>(v[2]), a3 = v[3] + dppf<ROR8>(v[3]);
  const float b0 = (lane & 8) ? a2 : a0, b1 = (lane & 8) ? a3 : a1;
  const auto t = __builtin_amdgcn_permlane16_swap(asu(b0), asu(b1), false, false);
  return sum32(asf(t[0]) + asf(t[1]));
}
__device__ __forceinline__ float ex2(float x) { return __builtin_amdgcn_exp2f(x); }
__device__ __forceinline__ void transpose8(const u32x4 (&in)[8], u32x4 (&out)[8]) {
#pragma unroll
  for (int k = 0; k < 4; ++k)
#pragma unroll
    for (int d = 0; d < 4; ++d) {
      out[2 * d][k] = __builtin_amdgcn_perm(in[2 * k + 1][d], in[2 * k][d], 0x05040100u);
      out[2 * d + 1][k] = __builtin_amdgcn_perm(in[2 * k + 1][d], in[2 * k][d], 0x07060302u);
    }
#pragma unroll
  for (int e = 0; e < 8; ++e) asm volatile("" : "+v"(out[e]));
}
#define RB(x) asf(pkbf(z0, (x)))
#define KEEP8(d) do { _Pragma("unroll") for (int e_ = 0; e_ < 8; ++e_) asm volatile("" :: "v"(d[e_])); } while (0)
#define FENCE() asm volatile("" ::: "memory")
#define BAR() do { asm volatile("s_waitcnt lgkmcnt(0)" ::: "memory"); __builtin_amdgcn_s_barrier(); asm volatile("" ::: "memory"); } while (0)
#define SB() __builtin_amdgcn_sched_barrier(0)
#define CODE_PF() do { u32 kp_, vt_; asm volatile("s_getpc_b64 s[88:89]\n1:\n\ts_sub_u32 s88, s88, 1b-blk_k_block\n\ts_subb_u32 s89, s89, 0\n\ts_and_b32 s89, s89, 0xffff\n\t" \
    "s_mov_b32 s90, 0x4900\n\ts_mov_b32 s91, 0x20000\n\tv_lshlrev_b32 %1, 7, %2\n\ts_mov_b32 %0, m0\n\ts_mov_b32 m0, %3\n\ts_nop 0\n\t" \
    "buffer_load_dword %1, s[88:91], 0 offen lds\n\ts_mov_b32 m0, %0" : "=&s"(kp_), "=&v"(vt_) : "v"(tid), "s"(lbase + L_PF * 4) : "s88", "s89", "s90", "s91", "scc", "memory"); } while (0)
#define LWAIT(i, v) do { _Pragma("unroll 1") for (u32 sp_ = 0; sp_ < (SPIN_CAP << 3); ++sp_) { \
    if ((u32)__builtin_amdgcn_readfirstlane(__hip_atomic_load(lds + L_CNT + (i), __ATOMIC_RELAXED, __HIP_MEMORY_SCOPE_WORKGROUP)) >= (v)) break; \
    __builtin_amdgcn_s_sleep(1); } FENCE(); } while (0)

extern "C" __global__ void __launch_bounds__(512, 2)
blk_k_block(const void* __restrict__ px, const void* __restrict__ pk, const void* __restrict__ pv, const void* __restrict__ pwq,
            const void* __restrict__ pwo, const void* __restrict__ pwg, const void* __restrict__ pwu, const void* __restrict__ pwd,
            void* __restrict__ pout, void* __restrict__ pws) {
  __shared__ __attribute__((aligned(16))) u32 lds[L_END];
  float* const ldf = reinterpret_cast<float*>(lds);
  const u32 lbase = (u32)(uintptr_t)(lds_u32*)lds;
  const int j = blockIdx.x, xcd = j & 7, ii = j >> 3;
  const int tid = threadIdx.x, lane = tid & 63, w = __builtin_amdgcn_readfirstlane(tid >> 6);
  const int r = lane >> 4, c = lane & 15;
  CODE_PF();
  asm volatile("" :: "s"(px), "s"(pk), "s"(pv), "s"(pwq), "s"(pwo), "s"(pwg), "s"(pwu), "s"(pwd), "s"(pout), "s"(pws));
  float z0 = 0.f;
  asm volatile("" : "+s"(z0));
  const rsrc_t rws = mk(pws, WS_END), rx = mk(px, 2 * DM * 2);
  const int bh = (ii >> 4) * 32 + 4 * xcd + ((ii >> 2) & 3), sp = ii & 3;
  const int ro = (ii & 3) * 8 + (ii >> 2);
  const int gn = ro - 10, gm = xcd + 8 * (gn >> 2);
  const bool isW = ro < 10, isG = gn >= 0 && gn < 16 && gm < 27, isF = ro >= 26 && ro < 31;
  const int wg = xcd ^ 1;
  const int gb = gn & 1, gc = 2 * gm + ((gn >> 1) & 1);
  const u32 vo = lane * 16;
  u32 tag = 0, tag_hi = 0;

  if (w == QW) {
    __builtin_amdgcn_s_setprio(3);
    const int qh = bh & 31, qk = (bh >> 5) * 4 + sp;
    u32x2 ep, xd;
    u32x4 xc[3], xa, xb, wqv[20];
    {
      const rsrc_t rwq = mk(pwq, DM * NH * DH * 2);
      u32 hb = (lane >> 5) * 1280 + (lane & 31) * 16, hb2 = (lane & 31) < 16 ? hb + 1024 : OOB;
      const u32 row0 = 80 * qk + 20 * r;
      u32 xo = ((lane >> 3) & 1) * 1280 + row0 * 2;
      u32 w0 = row0 * 8192 + qh * 256 + c * 16, w1 = w0 + 8192, w2 = w0 + 16384, w3 = w0 + 24576;
      u32 s1 = 32768, s2 = 65536, s3 = 98304, s4 = 131072;
      asm volatile("" : "+v"(hb), "+v"(hb2), "+v"(xo), "+v"(w0), "+v"(w1), "+v"(w2), "+v"(w3), "+s"(s1), "+s"(s2), "+s"(s3), "+s"(s4));
      if (lane < 4) lds[L_CNT + lane] = 0;
      SB();
      ep = ld8<AUX_SC1>(rws, WS_EPOCH, 0);
      xc[0] = ld16<0>(rx, hb, 0); xc[1] = ld16<0>(rx, hb + 512, 0); xc[2] = ld16<0>(rx, hb2, 0);
      xa = ld16<0>(rx, xo, 0); xb = ld16<0>(rx, xo + 16, 0); xd = ld8<0>(rx, xo + 32, 0);
      SB();
#pragma unroll
      for (int t = 0; t < 20; ++t) {
        wqv[t] = ld16<AUX_NT>(rwq, (t & 3) == 0 ? w0 : (t & 3) == 1 ? w1 : (t & 3) == 2 ? w2 : w3, t < 4 ? 0u : t < 8 ? s1 : t < 12 ? s2 : t < 16 ? s3 : s4);
        SB();
      }
    }
    FENCE();
    SB(); BAR(); SB();
    tag = (u32)__builtin_amdgcn_readfirstlane(ep[0]) + 1u;
    tag_hi = (u32)__builtin_amdgcn_readfirstlane(ep[1]) + (tag == 0u ? 1u : 0u);
    float s = 0.f;
#pragma unroll
    for (int k = 0; k < 3; ++k)
#pragma unroll
      for (int d = 0; d < 4; ++d) s += blo(xc[k][d]) + bhi(xc[k][d]);
    s = sum16(rowsum(s));
    const float mean = s * (1.f / DM);
    float v = 0.f;
#pragma unroll
    for (int k = 0; k < 3; ++k) {
      float vk = 0.f;
#pragma unroll
      for (int d = 0; d < 4; ++d) { const float a = blo(xc[k][d]) - mean, b2 = bhi(xc[k][d]) - mean; vk = fmaf(a, a, vk); vk = fmaf(b2, b2, vk); }
      v += (k < 2 || (lane & 31) < 16) ? vk : 0.f;
    }
    v = sum16(rowsum(v));
    const float mb = RB(mean), rinv = 1.f / RB(sqrtf(RB(RB(v * (1.f / DM)) + 1e-5f)));
    const float m0 = asf(__builtin_amdgcn_readlane(asu(mb), 0)), m1 = asf(__builtin_amdgcn_readlane(asu(mb), 32));
    const float i0 = asf(__builtin_amdgcn_readlane(asu(rinv), 0)), i1 = asf(__builtin_amdgcn_readlane(asu(rinv), 32));
    const bool b1 = (lane >> 3) & 1;
    const float mm = b1 ? m1 : m0, ri = b1 ? i1 : i0;
    const u32 xw[10] = {xa[0], xa[1], xa[2], xa[3], xb[0], xb[1], xb[2], xb[3], xd[0], xd[1]};
    u32 sl[4], sh[4];
#pragma unroll
    for (int k = 0; k < 4; ++k) {
      const bool on = ((lane & 7) >> 1) == k;
      sl[k] = on ? ((lane & 1) ? 0x01000c0cu : 0x0c0c0100u) : 0x0c0c0c0cu;
      sh[k] = on ? ((lane & 1) ? 0x03020c0cu : 0x0c0c0302u) : 0x0c0c0c0cu;
    }
    f32x4 qa = {0.f, 0.f, 0.f, 0.f}, qb = {0.f, 0.f, 0.f, 0.f};
#pragma unroll
    for (int t = 0; t < 10; ++t) {
      const u32 dp = pkbf(blo(xw[t]) - mm, bhi(xw[t]) - mm);
      const u32 xn = pkbf(blo(dp) * ri, bhi(dp) * ri);
      qa = mfma(perm4(xn, sl), wqv[2 * t], qa);
      qb = mfma(perm4(xn, sh), wqv[2 * t + 1], qb);
    }
    qa += qb;
    FENCE();
    {
      u32x4 g0 = {asu(qa[0]), tag, asu(qa[1]), tag}, g1 = {asu(qa[2]), tag, asu(qa[3]), tag};
      u32 ga = WS_QG + ((qh * 2 + (lane >> 5)) * 8 + qk) * 1024 + (lane & 31) * 16;
      asm volatile("" : "+v"(g0), "+v"(g1), "+v"(ga));
      SB();
      st16(rws, ga, g0); st16(rws, ga + 512, g1);
      FENCE(); SB();
    }
    u32 qb0 = WS_QG + (u32)(qh * 2 + (bh >> 5)) * 8192u, qb1 = qb0 + 4096u;
    asm volatile("" : "+s"(qb0), "+s"(qb1));
    u32x4 g[8];
#pragma unroll 1
    for (u32 spins = 0; spins < SPIN_CAP; ++spins) {
      FENCE(); SB();
      bool ok = true;
#pragma unroll
      for (int k = 0; k < 8; ++k) g[k] = ld16<AUX_SC1>(rws, vo + (k & 3) * 1024, k < 4 ? qb0 : qb1);
      SB();
      if (lane == 0) __hip_atomic_store(lds + L_CNT + 2, 1u, __ATOMIC_RELAXED, __HIP_MEMORY_SCOPE_WORKGROUP);
      SB();
#pragma unroll
      for (int k = 0; k < 8; ++k) ok &= (g[k][1] == tag) & (g[k][3] == tag);
      if (__all(ok)) break;
    }
    float q0 = 0.f, q1 = 0.f;
#pragma unroll
    for (int k = 0; k < 8; ++k) { q0 += asf(g[k][0]); q1 += asf(g[k][2]); }
    lds[L_QS + 4 * c + 2 * (r & 1) + (r >> 1)] = pkbf(q0, q1);
    if (lane == 0) *reinterpret_cast<u32x2*>(lds + L_TAG) = u32x2{tag, tag_hi};
    asm volatile("s_waitcnt lgkmcnt(0)" ::: "memory");
    if (lane == 0) __hip_atomic_store(lds + L_CNT + 2, 2u, __ATOMIC_RELAXED, __HIP_MEMORY_SCOPE_WORKGROUP);
    const u32x4 sent4 = {SENT, SENT, SENT, SENT};
    const u32 fa = WS_FP + (gb * 54 + gc) * 2560 + vo;
    st16(rws, lane < 33 ? WS_R + (bh * 4 + sp) * 640 + vo : OOB, sent4);
    st16(rws, isW ? (lane < 32 ? WS_Y + (r * 8 + wg) * 2560 + ro * 256 + c * 16 : OOB) : (isG ? fa : OOB), sent4);
    st16(rws, isG ? fa + 1024 : OOB, sent4);
    st16(rws, isG && lane < 32 ? fa + 2048 : OOB, sent4);
    asm volatile("s_waitcnt vmcnt(0)" ::: "memory");
    st8(rws, lane == 0 ? WS_FLAG + j * 8 : OOB, u32x2{tag, tag_hi});
    __builtin_amdgcn_s_setprio(0);
  }
  FENCE(); SB();

  {
    u32 psel[4];
    const u32 src = (lane & 8) ? 0x0302u : 0x0100u;
    const u32 one = (lane & 1) ? ((src << 16) | 0x0c0cu) : (0x0c0c0000u | src);
#pragma unroll
    for (int k = 0; k < 4; ++k) psel[k] = (((lane & 7) >> 1) == k) ? one : 0x0c0c0c0cu;
    u32 rn = (w < 4 ? w : (w == 4 ? 124 : 59 + w)) + (((((u32)bh * 2654435761u) >> 10) & 31u) << 2);
    const u32 rdn = w < 4 ? 4 : (w == 4 ? 1 : 3);
    u32 cyc = w < 4 ? 3 : (w == 4 ? 0 : 4);
    const u32 sig = (u32)((w + 3) >> 3);
    const u32 kvb = (u32)(bh * SK * 256) + (u32)sp * 16384u;
    const u32x4 dk = desc(pk, 2 * NH * SK * DH * 2), dv = desc(pv, 2 * NH * SK * DH * 2), dw = desc(pws, WS_END);
    const u32 a_lf = lbase + (L_CNT + 2) * 4, a_lc = lbase + L_CNT * 4, a_lq = lbase + (L_QS + 4 * c) * 4;
    const u32 a_lm = lbase + (L_ML + 2 * w) * 4, a_lr = lbase + (L_MG + w * 256 + lane * 4) * 4, a_l8 = lbase + (L_ML + 2 * (lane & 7)) * 4, a_lb = lbase + (L_MG + lane * 4) * 4;
    const u32 a_ra = lane <= 32 ? WS_R + (bh * 4 + sp) * 640 + (lane < 32 ? c * 32 + r * 16 : 512) : OOB;
    asm volatile(
    "s_mov_b32 s88, 0xff00ff00\n\t"
    "s_mov_b32 s89, 0xff00ff00\n\t"
    "s_mov_b32 s90, 0xf0f0f0f0\n\t"
    "s_mov_b32 s91, 0xf0f0f0f0\n\t"
    "s_cmp_eq_u32 %[rdn], 1\n\t"
    "s_cbranch_scc1 9f\n\t"
    "s_barrier\n\t"
    "s_cmp_lg_u32 %[wv], 0\n\t"
    "s_cbranch_scc1 9f\n\t"
    "s_mov_b32 s94, 0x1000000\n\t"
    "6:\n\t"
    "ds_read_b32 v72, %[lf]\n\t"
    "s_waitcnt lgkmcnt(0)\n\t"
    "v_readfirstlane_b32 s92, v72\n\t"
    "s_cmp_ge_u32 s92, 1\n\t"
    "s_cbranch_scc1 7f\n\t"
    "s_sleep 1\n\t"
    "s_sub_u32 s94, s94, 1\n\t"
    "s_cmp_lg_u32 s94, 0\n\t"
    "s_cbranch_scc1 6b\n\t"
    "7:\n\t"
    "9:\n\t"
    "s_bfe_u32 s92, %[rn], 0x50002\n\t"
    "s_and_b32 s93, %[rn], 3\n\t"
    "s_lshl_b32 s92, s92, 16\n\t"
    "s_lshl_b32 s93, s93, 12\n\t"
    "s_add_u32 s92, s92, %[kvb]\n\t"
    "s_add_u32 %[rn], %[rn], %[rdn]\n\t"
    "s_add_u32 s92, s92, s93\n\t"
    "buffer_load_dwordx4 v[96:99], %[vo], %[rk], s92 offen nt\n\t"
    "buffer_load_dwordx4 v[100:103], %[vo], %[rk], s92 offen offset:1024 nt\n\t"
    "buffer_load_dwordx4 v[104:107], %[vo], %[rk], s92 offen offset:2048 nt\n\t"
    "buffer_load_dwordx4 v[108:111], %[vo], %[rk], s92 offen offset:3072 nt\n\t"
    "buffer_load_dwordx4 v[112:115], %[vo], %[rv], s92 offen nt\n\t"
    "buffer_load_dwordx4 v[116:119], %[vo], %[rv], s92 offen offset:1024 nt\n\t"
    "buffer_load_dwordx4 v[120:123], %[vo], %[rv], s92 offen offset:2048 nt\n\t"
    "buffer_load_dwordx4 v[124:127], %[vo], %[rv], s92 offen offset:3072 nt\n\t"
    "s_bfe_u32 s92, %[rn], 0x50002\n\t"
    "s_and_b32 s93, %[rn], 3\n\t"
    "s_lshl_b32 s92, s92, 16\n\t"
    "s_lshl_b32 s93, s93, 12\n\t"
    "s_add_u32 s92, s92, %[kvb]\n\t"
    "s_add_u32 %[rn], %[rn], %[rdn]\n\t"
    "s_add_u32 s92, s92, s93\n\t"
    "buffer_load_dwordx4 v[128:131], %[vo], %[rk], s92 offen nt\n\t"
    "buffer_load_dwordx4 v[132:135], %[vo], %[rk], s92 offen offset:1024 nt\n\t"
    "buffer_load_dwordx4 v[136:139], %[vo], %[rk], s92 offen offset:2048 nt\n\t"
    "buffer_load_dwordx4 v[140:143], %[vo], %[rk], s92 offen offset:3072 nt\n\t"
    "buffer_load_dwordx4 v[144:147], %[vo], %[rv], s92 offen nt\n\t"
    "buffer_load_dwordx4 v[148:151], %[vo], %[rv], s92 offen offset:1024 nt\n\t"
    "buffer_load_dwordx4 v[152:155], %[vo], %[rv], s92 offen offset:2048 nt\n\t"
    "buffer_load_dwordx4 v[156:159], %[vo], %[rv], s92 offen offset:3072 nt\n\t"
    "s_bfe_u32 s92, %[rn], 0x50002\n\t"
    "s_and_b32 s93, %[rn], 3\n\t"
    "s_lshl_b32 s92, s92, 16\n\t"
    "s_lshl_b32 s93, s93, 12\n\t"
    "s_add_u32 s92, s92, %[kvb]\n\t"
    "s_add_u32 %[rn], %[rn], %[rdn]\n\t"
    "s_add_u32 s92, s92, s93\n\t"
    "buffer_load_dwordx4 v[160:163], %[vo], %[rk], s92 offen nt\n\t"
    "buffer_load_dwordx4 v[164:167], %[vo], %[rk], s92 offen offset:1024 nt\n\t"
    "buffer_load_dwordx4 v[168:171], %[vo], %[rk], s92 offen offset:2048 nt\n\t"
    "buffer_load_dwordx4 v[172:175], %[vo], %[rk], s92 offen offset:3072 nt\n\t"
    "buffer_load_dwordx4 v[176:179], %[vo], %[rv], s92 offen nt\n\t"
    "buffer_load_dwordx4 v[180:183], %[vo], %[rv], s92 offen offset:1024 nt\n\t"
    "buffer_load_dwordx4 v[184:187], %[vo], %[rv], s92 offen offset:2048 nt\n\t"
    "buffer_load_dwordx4 v[188:191], %[vo], %[rv], s92 offen offset:3072 nt\n\t"
    "s_bfe_u32 s92, %[rn], 0x50002\n\t"
    "s_and_b32 s93, %[rn], 3\n\t"
    "s_lshl_b32 s92, s92, 16\n\t"
    "s_lshl_b32 s93, s93, 12\n\t"
    "s_add_u32 s92, s92, %[kvb]\n\t"
    "s_add_u32 %[rn], %[rn], %[rdn]\n\t"
    "s_add_u32 s92, s92, s93\n\t"
    "buffer_load_dwordx4 v[192:195], %[vo], %[rk], s92 offen nt\n\t"
    "buffer_load_dwordx4 v[196:199], %[vo], %[rk], s92 offen offset:1024 nt\n\t"
    "buffer_load_dwordx4 v[200:203], %[vo], %[rk], s92 offen offset:2048 nt\n\t"
    "buffer_load_dwordx4 v[204:207], %[vo], %[rk], s92 offen offset:3072 nt\n\t"
    "buffer_load_dwordx4 v[208:211], %[vo], %[rv], s92 offen nt\n\t"
    "buffer_load_dwordx4 v[212:215], %[vo], %[rv], s92 offen offset:1024 nt\n\t"
    "buffer_load_dwordx4 v[216:219], %[vo], %[rv], s92 offen offset:2048 nt\n\t"
    "buffer_load_dwordx4 v[220:223], %[vo], %[rv], s92 offen offset:3072 nt\n\t"
    "s_mov_b32 s94, 0x1000000\n\t"
    "20:\n\t"
    "ds_read_b32 v72, %[lf]\n\t"
    "s_waitcnt lgkmcnt(0)\n\t"
    "v_readfirstlane_b32 s92, v72\n\t"
    "s_cmp_ge_u32 s92, 2\n\t"
    "s_cbranch_scc1 21f\n\t"
    "s_sleep 1\n\t"
    "s_sub_u32 s94, s94, 1\n\t"
    "s_cmp_lg_u32 s94, 0\n\t"
    "s_cbranch_scc1 20b\n\t"
    "21:\n\t"
    "ds_read_b128 v[64:67], %[lq]\n\t"
    "v_mov_b32 v58, 0xff800000\n\t"
    "v_mov_b32 v59, 0\n\t"
    "v_mov_b32 v60, 0\n\t"
    "v_mov_b32 v61, 0\n\t"
    "v_mov_b32 v62, 0\n\t"
    "v_mov_b32 v63, 0\n\t"
    "v_mov_b32 v79, 1\n\t"
    "s_waitcnt lgkmcnt(0)\n\t"
    "s_cmp_eq_u32 %[it], 0\n\t"
    "s_cbranch_scc1 40f\n\t"
    "30:\n\t"
    "s_waitcnt vmcnt(30)\n\t"
    "v_dot2_f32_bf16 v68, v96, v64, 0\n\t"
    "v_dot2_f32_bf16 v69, v100, v64, 0\n\t"
    "s_waitcnt vmcnt(28)\n\t"
    "v_dot2_f32_bf16 v70, v104, v64, 0\n\t"
    "v_dot2_f32_bf16 v71, v108, v64, 0\n\t"
    "v_dot2_f32_bf16 v68, v97, v65, v68\n\t"
    "v_dot2_f32_bf16 v69, v101, v65, v69\n\t"
    "v_dot2_f32_bf16 v70, v105, v65, v70\n\t"
    "v_dot2_f32_bf16 v71, v109, v65, v71\n\t"
    "v_dot2_f32_bf16 v68, v98, v66, v68\n\t"
    "v_dot2_f32_bf16 v69, v102, v66, v69\n\t"
    "v_dot2_f32_bf16 v70, v106, v66, v70\n\t"
    "v_dot2_f32_bf16 v71, v110, v66, v71\n\t"
    "v_dot2_f32_bf16 v68, v99, v67, v68\n\t"
    "v_dot2_f32_bf16 v69, v103, v67, v69\n\t"
    "v_dot2_f32_bf16 v70, v107, v67, v70\n\t"
    "v_dot2_f32_bf16 v71, v111, v67, v71\n\t"
    "v_add_f32_dpp v68, v68, v68 row_ror:8 row_mask:0xf bank_mask:0xf bound_ctrl:1\n\t"
    "v_add_f32_dpp v69, v69, v69 row_ror:8 row_mask:0xf bank_mask:0xf bound_ctrl:1\n\t"
    "v_add_f32_dpp v70, v70, v70 row_ror:8 row_mask:0xf bank_mask:0xf bound_ctrl:1\n\t"
    "v_add_f32_dpp v71, v71, v71 row_ror:8 row_mask:0xf bank_mask:0xf bound_ctrl:1\n\t"
    "v_cndmask_b32_e64 v72, v68, v70, s[88:89]\n\t"
    "v_cndmask_b32_e64 v73, v69, v71, s[88:89]\n\t"
    "s_nop 0\n\t"
    "v_add_f32_dpp v72, v72, v72 row_half_mirror row_mask:0xf bank_mask:0xf bound_ctrl:1\n\t"
    "v_add_f32_dpp v73, v73, v73 row_half_mirror row_mask:0xf bank_mask:0xf bound_ctrl:1\n\t"
    "v_cndmask_b32_e64 v74, v72, v73, s[90:91]\n\t"
    "s_nop 1\n\t"
    "v_add_f32_dpp v74, v74, v74 quad_perm:[2,3,0,1] row_mask:0xf bank_mask:0xf bound_ctrl:1\n\t"
    "s_nop 1\n\t"
    "v_add_f32_dpp v74, v74, v74 quad_perm:[1,0,3,2] row_mask:0xf bank_mask:0xf bound_ctrl:1\n\t"
    "v_cvt_pk_bf16_f32 v74, 0, v74\n\t"
    "v_mul_f32 v74, 0x3db504f3, v74\n\t"
    "v_cvt_pk_bf16_f32 v74, 0, v74\n\t"
    "s_nop 1\n\t"
    "v_max_f32_dpp v75, v74, v74 row_ror:4 row_mask:0xf bank_mask:0xf bound_ctrl:1\n\t"
    "s_nop 1\n\t"
    "v_max_f32_dpp v75, v75, v75 row_ror:8 row_mask:0xf bank_mask:0xf bound_ctrl:1\n\t"
    "v_mov_b32 v76, v75\n\t"
    "s_nop 1\n\t"
    "v_permlane16_swap_b32 v75, v76\n\t"
    "v_max_f32 v75, v75, v76\n\t"
    "v_mov_b32 v76, v75\n\t"
    "s_nop 1\n\t"
    "v_permlane32_swap_b32 v75, v76\n\t"
    "v_max3_f32 v75, v58, v75, v76\n\t"
    "v_sub_f32 v77, v74, v75\n\t"
    "v_sub_f32 v78, v58, v75\n\t"
    "v_mul_f32 v77, 0x3fb8aa3b, v77\n\t"
    "v_mul_f32 v78, 0x3fb8aa3b, v78\n\t"
    "v_exp_f32 v77, v77\n\t"
    "v_exp_f32 v78, v78\n\t"
    "v_mov_b32 v58, v75\n\t"
    "v_cvt_pk_bf16_f32 v76, 0, v77\n\t"
    "v_fma_f32 v59, v59, v78, v77\n\t"
    "v_sub_f32 v76, v77, v76\n\t"
    "v_cvt_pk_bf16_f32 v76, v77, v76\n\t"
    "v_pk_mul_f32 v[60:61], v[60:61], v[78:79] op_sel_hi:[1,0]\n\t"
    "v_pk_mul_f32 v[62:63], v[62:63], v[78:79] op_sel_hi:[1,0]\n\t"
    "v_mov_b32_dpp v68, v76 row_newbcast:0 row_mask:0xf bank_mask:0xf bound_ctrl:1\n\t"
    "v_mov_b32_dpp v69, v76 row_newbcast:4 row_mask:0xf bank_mask:0xf bound_ctrl:1\n\t"
    "v_mov_b32_dpp v70, v76 row_newbcast:8 row_mask:0xf bank_mask:0xf bound_ctrl:1\n\t"
    "v_mov_b32_dpp v71, v76 row_newbcast:12 row_mask:0xf bank_mask:0xf bound_ctrl:1\n\t"
    "v_perm_b32 v80, v68, v68, %[p0]\n\t"
    "v_perm_b32 v81, v68, v68, %[p1]\n\t"
    "v_perm_b32 v82, v68, v68, %[p2]\n\t"
    "v_perm_b32 v83, v68, v68, %[p3]\n\t"
    "v_perm_b32 v84, v69, v69, %[p0]\n\t"
    "v_perm_b32 v85, v69, v69, %[p1]\n\t"
    "v_perm_b32 v86, v69, v69, %[p2]\n\t"
    "v_perm_b32 v87, v69, v69, %[p3]\n\t"
    "v_perm_b32 v88, v70, v70, %[p0]\n\t"
    "v_perm_b32 v89, v70, v70, %[p1]\n\t"
    "v_perm_b32 v90, v70, v70, %[p2]\n\t"
    "v_perm_b32 v91, v70, v70, %[p3]\n\t"
    "v_perm_b32 v92, v71, v71, %[p0]\n\t"
    "v_perm_b32 v93, v71, v71, %[p1]\n\t"
    "v_perm_b32 v94, v71, v71, %[p2]\n\t"
    "v_perm_b32 v95, v71, v71, %[p3]\n\t"
    "s_waitcnt vmcnt(26)\n\t"
    "v_mfma_f32_16x16x32_bf16 v[60:63], v[80:83], v[112:115], v[60:63]\n\t"
    "v_mfma_f32_16x16x32_bf16 v[60:63], v[84:87], v[116:119], v[60:63]\n\t"
    "s_waitcnt vmcnt(24)\n\t"
    "v_mfma_f32_16x16x32_bf16 v[60:63], v[88:91], v[120:123], v[60:63]\n\t"
    "v_mfma_f32_16x16x32_bf16 v[60:63], v[92:95], v[124:127], v[60:63]\n\t"
    "s_bfe_u32 s92, %[rn], 0x50002\n\t"
    "s_and_b32 s93, %[rn], 3\n\t"
    "s_lshl_b32 s92, s92, 16\n\t"
    "s_lshl_b32 s93, s93, 12\n\t"
    "s_add_u32 s92, s92, %[kvb]\n\t"
    "s_add_u32 %[rn], %[rn], %[rdn]\n\t"
    "s_add_u32 s92, s92, s93\n\t"
    "buffer_load_dwordx4 v[96:99], %[vo], %[rk], s92 offen nt\n\t"
    "buffer_load_dwordx4 v[100:103], %[vo], %[rk], s92 offen offset:1024 nt\n\t"
    "buffer_load_dwordx4 v[104:107], %[vo], %[rk], s92 offen offset:2048 nt\n\t"
    "buffer_load_dwordx4 v[108:111], %[vo], %[rk], s92 offen offset:3072 nt\n\t"
    "buffer_load_dwordx4 v[112:115], %[vo], %[rv], s92 offen nt\n\t"
    "buffer_load_dwordx4 v[116:119], %[vo], %[rv], s92 offen offset:1024 nt\n\t"
    "buffer_load_dwordx4 v[120:123], %[vo], %[rv], s92 offen offset:2048 nt\n\t"
    "buffer_load_dwordx4 v[124:127], %[vo], %[rv], s92 offen offset:3072 nt\n\t"
    "s_waitcnt vmcnt(30)\n\t"
    "v_dot2_f32_bf16 v68, v128, v64, 0\n\t"
    "v_dot2_f32_bf16 v69, v132, v64, 0\n\t"
    "s_waitcnt vmcnt(28)\n\t"
    "v_dot2_f32_bf16 v70, v136, v64, 0\n\t"
    "v_dot2_f32_bf16 v71, v140, v64, 0\n\t"
    "v_dot2_f32_bf16 v68, v129, v65, v68\n\t"
    "v_dot2_f32_bf16 v69, v133, v65, v69\n\t"
    "v_dot2_f32_bf16 v70, v137, v65, v70\n\t"
    "v_dot2_f32_bf16 v71, v141, v65, v71\n\t"
    "v_dot2_f32_bf16 v68, v130, v66, v68\n\t"
    "v_dot2_f32_bf16 v69, v134, v66, v69\n\t"
    "v_dot2_f32_bf16 v70, v138, v66, v70\n\t"
    "v_dot2_f32_bf16 v71, v142, v66, v71\n\t"
    "v_dot2_f32_bf16 v68, v131, v67, v68\n\t"
    "v_dot2_f32_bf16 v69, v135, v67, v69\n\t"
    "v_dot2_f32_bf16 v70, v139, v67, v70\n\t"
    "v_dot2_f32_bf16 v71, v143, v67, v71\n\t"
    "v_add_f32_dpp v68, v68, v68 row_ror:8 row_mask:0xf bank_mask:0xf bound_ctrl:1\n\t"
    "v_add_f32_dpp v69, v69, v69 row_ror:8 row_mask:0xf bank_mask:0xf bound_ctrl:1\n\t"
    "v_add_f32_dpp v70, v70, v70 row_ror:8 row_mask:0xf bank_mask:0xf bound_ctrl:1\n\t"
    "v_add_f32_dpp v71, v71, v71 row_ror:8 row_mask:0xf bank_mask:0xf bound_ctrl:1\n\t"
    "v_cndmask_b32_e64 v72, v68, v70, s[88:89]\n\t"
    "v_cndmask_b32_e64 v73, v69, v71, s[88:89]\n\t"
    "s_nop 0\n\t"
    "v_add_f32_dpp v72, v72, v72 row_half_mirror row_mask:0xf bank_mask:0xf bound_ctrl:1\n\t"
    "v_add_f32_dpp v73, v73, v73 row_half_mirror row_mask:0xf bank_mask:0xf bound_ctrl:1\n\t"
    "v_cndmask_b32_e64 v74, v72, v73, s[90:91]\n\t"
    "s_nop 1\n\t"
    "v_add_f32_dpp v74, v74, v74 quad_perm:[2,3,0,1] row_mask:0xf bank_mask:0xf bound_ctrl:1\n\t"
    "s_nop 1\n\t"
    "v_add_f32_dpp v74, v74, v74 quad_perm:[1,0,3,2] row_mask:0xf bank_mask:0xf bound_ctrl:1\n\t"
    "v_cvt_pk_bf16_f32 v74, 0, v74\n\t"
    "v_mul_f32 v74, 0x3db504f3, v74\n\t"
    "v_cvt_pk_bf16_f32 v74, 0, v74\n\t"
    "s_nop 1\n\t"
    "v_max_f32_dpp v75, v74, v74 row_ror:4 row_mask:0xf bank_mask:0xf bound_ctrl:1\n\t"
    "s_nop 1\n\t"
    "v_max_f32_dpp v75, v75, v75 row_ror:8 row_mask:0xf bank_mask:0xf bound_ctrl:1\n\t"
    "v_mov_b32 v76, v75\n\t"
    "s_nop 1\n\t"
    "v_permlane16_swap_b32 v75, v76\n\t"
    "v_max_f32 v75, v75, v76\n\t"
    "v_mov_b32 v76, v75\n\t"
    "s_nop 1\n\t"
    "v_permlane32_swap_b32 v75, v76\n\t"
    "v_max3_f32 v75, v58, v75, v76\n\t"
    "v_sub_f32 v77, v74, v75\n\t"
    "v_sub_f32 v78, v58, v75\n\t"
    "v_mul_f32 v77, 0x3fb8aa3b, v77\n\t"
    "v_mul_f32 v78, 0x3fb8aa3b, v78\n\t"
    "v_exp_f32 v77, v77\n\t"
    "v_exp_f32 v78, v78\n\t"
    "v_mov_b32 v58, v75\n\t"
    "v_cvt_pk_bf16_f32 v76, 0, v77\n\t"
    "v_fma_f32 v59, v59, v78, v77\n\t"
    "v_sub_f32 v76, v77, v76\n\t"
    "v_cvt_pk_bf16_f32 v76, v77, v76\n\t"
    "v_pk_mul_f32 v[60:61], v[60:61], v[78:79] op_sel_hi:[1,0]\n\t"
    "v_pk_mul_f32 v[62:63], v[62:63], v[78:79] op_sel_hi:[1,0]\n\t"
    "v_mov_b32_dpp v68, v76 row_newbcast:0 row_mask:0xf bank_mask:0xf bound_ctrl:1\n\t"
    "v_mov_b32_dpp v69, v76 row_newbcast:4 row_mask:0xf bank_mask:0xf bound_ctrl:1\n\t"
    "v_mov_b32_dpp v70, v76 row_newbcast:8 row_mask:0xf bank_mask:0xf bound_ctrl:1\n\t"
    "v_mov_b32_dpp v71, v76 row_newbcast:12 row_mask:0xf bank_mask:0xf bound_ctrl:1\n\t"
    "v_perm_b32 v80, v68, v68, %[p0]\n\t"
    "v_perm_b32 v81, v68, v68, %[p1]\n\t"
    "v_perm_b32 v82, v68, v68, %[p2]\n\t"
    "v_perm_b32 v83, v68, v68, %[p3]\n\t"
    "v_perm_b32 v84, v69, v69, %[p0]\n\t"
    "v_perm_b32 v85, v69, v69, %[p1]\n\t"
    "v_perm_b32 v86, v69, v69, %[p2]\n\t"
    "v_perm_b32 v87, v69, v69, %[p3]\n\t"
    "v_perm_b32 v88, v70, v70, %[p0]\n\t"
    "v_perm_b32 v89, v70, v70, %[p1]\n\t"
    "v_perm_b32 v90, v70, v70, %[p2]\n\t"
    "v_perm_b32 v91, v70, v70, %[p3]\n\t"
    "v_perm_b32 v92, v71, v71, %[p0]\n\t"
    "v_perm_b32 v93, v71, v71, %[p1]\n\t"
    "v_perm_b32 v94, v71, v71, %[p2]\n\t"
    "v_perm_b32 v95, v71, v71, %[p3]\n\t"
    "s_waitcnt vmcnt(26)\n\t"
    "v_mfma_f32_16x16x32_bf16 v[60:63], v[80:83], v[144:147], v[60:63]\n\t"
    "v_mfma_f32_16x16x32_bf16 v[60:63], v[84:87], v[148:151], v[60:63]\n\t"
    "s_waitcnt vmcnt(24)\n\t"
    "v_mfma_f32_16x16x32_bf16 v[60:63], v[88:91], v[152:155], v[60:63]\n\t"
    "v_mfma_f32_16x16x32_bf16 v[60:63], v[92:95], v[156:159], v[60:63]\n\t"
    "s_bfe_u32 s92, %[rn], 0x50002\n\t"
    "s_and_b32 s93, %[rn], 3\n\t"
    "s_lshl_b32 s92, s92, 16\n\t"
    "s_lshl_b32 s93, s93, 12\n\t"
    "s_add_u32 s92, s92, %[kvb]\n\t"
    "s_add_u32 %[rn], %[rn], %[rdn]\n\t"
    "s_add_u32 s92, s92, s93\n\t"
    "buffer_load_dwordx4 v[128:131], %[vo], %[rk], s92 offen nt\n\t"
    "buffer_load_dwordx4 v[132:135], %[vo], %[rk], s92 offen offset:1024 nt\n\t"
    "buffer_load_dwordx4 v[136:139], %[vo], %[rk], s92 offen offset:2048 nt\n\t"
    "buffer_load_dwordx4 v[140:143], %[vo], %[rk], s92 offen offset:3072 nt\n\t"
    "buffer_load_dwordx4 v[144:147], %[vo], %[rv], s92 offen nt\n\t"
    "buffer_load_dwordx4 v[148:151], %[vo], %[rv], s92 offen offset:1024 nt\n\t"
    "buffer_load_dwordx4 v[152:155], %[vo], %[rv], s92 offen offset:2048 nt\n\t"
    "buffer_load_dwordx4 v[156:159], %[vo], %[rv], s92 offen offset:3072 nt\n\t"
    "s_waitcnt vmcnt(30)\n\t"
    "v_dot2_f32_bf16 v68, v160, v64, 0\n\t"
    "v_dot2_f32_bf16 v69, v164, v64, 0\n\t"
    "s_waitcnt vmcnt(28)\n\t"
    "v_dot2_f32_bf16 v70, v168, v64, 0\n\t"
    "v_dot2_f32_bf16 v71, v172, v64, 0\n\t"
    "v_dot2_f32_bf16 v68, v161, v65, v68\n\t"
    "v_dot2_f32_bf16 v69, v165, v65, v69\n\t"
    "v_dot2_f32_bf16 v70, v169, v65, v70\n\t"
    "v_dot2_f32_bf16 v71, v173, v65, v71\n\t"
    "v_dot2_f32_bf16 v68, v162, v66, v68\n\t"
    "v_dot2_f32_bf16 v69, v166, v66, v69\n\t"
    "v_dot2_f32_bf16 v70, v170, v66, v70\n\t"
    "v_dot2_f32_bf16 v71, v174, v66, v71\n\t"
    "v_dot2_f32_bf16 v68, v163, v67, v68\n\t"
    "v_dot2_f32_bf16 v69, v167, v67, v69\n\t"
    "v_dot2_f32_bf16 v70, v171, v67, v70\n\t"
    "v_dot2_f32_bf16 v71, v175, v67, v71\n\t"
    "v_add_f32_dpp v68, v68, v68 row_ror:8 row_mask:0xf bank_mask:0xf bound_ctrl:1\n\t"
    "v_add_f32_dpp v69, v69, v69 row_ror:8 row_mask:0xf bank_mask:0xf bound_ctrl:1\n\t"
    "v_add_f32_dpp v70, v70, v70 row_ror:8 row_mask:0xf bank_mask:0xf bound_ctrl:1\n\t"
    "v_add_f32_dpp v71, v71, v71 row_ror:8 row_mask:0xf bank_mask:0xf bound_ctrl:1\n\t"
    "v_cndmask_b32_e64 v72, v68, v70, s[88:89]\n\t"
    "v_cndmask_b32_e64 v73, v69, v71, s[88:89]\n\t"
    "s_nop 0\n\t"
    "v_add_f32_dpp v72, v72, v72 row_half_mirror row_mask:0xf bank_mask:0xf bound_ctrl:1\n\t"
    "v_add_f32_dpp v73, v73, v73 row_half_mirror row_mask:0xf bank_mask:0xf bound_ctrl:1\n\t"
    "v_cndmask_b32_e64 v74, v72, v73, s[90:91]\n\t"
    "s_nop 1\n\t"
    "v_add_f32_dpp v74, v74, v74 quad_perm:[2,3,0,1] row_mask:0xf bank_mask:0xf bound_ctrl:1\n\t"
    "s_nop 1\n\t"
    "v_add_f32_dpp v74, v74, v74 quad_perm:[1,0,3,2] row_mask:0xf bank_mask:0xf bound_ctrl:1\n\t"
    "v_cvt_pk_bf16_f32 v74, 0, v74\n\t"
    "v_mul_f32 v74, 0x3db504f3, v74\n\t"
    "v_cvt_pk_bf16_f32 v74, 0, v74\n\t"
    "s_nop 1\n\t"
    "v_max_f32_dpp v75, v74, v74 row_ror:4 row_mask:0xf bank_mask:0xf bound_ctrl:1\n\t"
    "s_nop 1\n\t"
    "v_max_f32_dpp v75, v75, v75 row_ror:8 row_mask:0xf bank_mask:0xf bound_ctrl:1\n\t"
    "v_mov_b32 v76, v75\n\t"
    "s_nop 1\n\t"
    "v_permlane16_swap_b32 v75, v76\n\t"
    "v_max_f32 v75, v75, v76\n\t"
    "v_mov_b32 v76, v75\n\t"
    "s_nop 1\n\t"
    "v_permlane32_swap_b32 v75, v76\n\t"
    "v_max3_f32 v75, v58, v75, v76\n\t"
    "v_sub_f32 v77, v74, v75\n\t"
    "v_sub_f32 v78, v58, v75\n\t"
    "v_mul_f32 v77, 0x3fb8aa3b, v77\n\t"
    "v_mul_f32 v78, 0x3fb8aa3b, v78\n\t"
    "v_exp_f32 v77, v77\n\t"
    "v_exp_f32 v78, v78\n\t"
    "v_mov_b32 v58, v75\n\t"
    "v_cvt_pk_bf16_f32 v76, 0, v77\n\t"
    "v_fma_f32 v59, v59, v78, v77\n\t"
    "v_sub_f32 v76, v77, v76\n\t"
    "v_cvt_pk_bf16_f32 v76, v77, v76\n\t"
    "v_pk_mul_f32 v[60:61], v[60:61], v[78:79] op_sel_hi:[1,0]\n\t"
    "v_pk_mul_f32 v[62:63], v[62:63], v[78:79] op_sel_hi:[1,0]\n\t"
    "v_mov_b32_dpp v68, v76 row_newbcast:0 row_mask:0xf bank_mask:0xf bound_ctrl:1\n\t"
    "v_mov_b32_dpp v69, v76 row_newbcast:4 row_mask:0xf bank_mask:0xf bound_ctrl:1\n\t"
    "v_mov_b32_dpp v70, v76 row_newbcast:8 row_mask:0xf bank_mask:0xf bound_ctrl:1\n\t"
    "v_mov_b32_dpp v71, v76 row_newbcast:12 row_mask:0xf bank_mask:0xf bound_ctrl:1\n\t"
    "v_perm_b32 v80, v68, v68, %[p0]\n\t"
    "v_perm_b32 v81, v68, v68, %[p1]\n\t"
    "v_perm_b32 v82, v68, v68, %[p2]\n\t"
    "v_perm_b32 v83, v68, v68, %[p3]\n\t"
    "v_perm_b32 v84, v69, v69, %[p0]\n\t"
    "v_perm_b32 v85, v69, v69, %[p1]\n\t"
    "v_perm_b32 v86, v69, v69, %[p2]\n\t"
    "v_perm_b32 v87, v69, v69, %[p3]\n\t"
    "v_perm_b32 v88, v70, v70, %[p0]\n\t"
    "v_perm_b32 v89, v70, v70, %[p1]\n\t"
    "v_perm_b32 v90, v70, v70, %[p2]\n\t"
    "v_perm_b32 v91, v70, v70, %[p3]\n\t"
    "v_perm_b32 v92, v71, v71, %[p0]\n\t"
    "v_perm_b32 v93, v71, v71, %[p1]\n\t"
    "v_perm_b32 v94, v71, v71, %[p2]\n\t"
    "v_perm_b32 v95, v71, v71, %[p3]\n\t"
    "s_waitcnt vmcnt(26)\n\t"
    "v_mfma_f32_16x16x32_bf16 v[60:63], v[80:83], v[176:179], v[60:63]\n\t"
    "v_mfma_f32_16x16x32_bf16 v[60:63], v[84:87], v[180:183], v[60:63]\n\t"
    "s_waitcnt vmcnt(24)\n\t"
    "v_mfma_f32_16x16x32_bf16 v[60:63], v[88:91], v[184:187], v[60:63]\n\t"
    "v_mfma_f32_16x16x32_bf16 v[60:63], v[92:95], v[188:191], v[60:63]\n\t"
    "s_bfe_u32 s92, %[rn], 0x50002\n\t"
    "s_and_b32 s93, %[rn], 3\n\t"
    "s_lshl_b32 s92, s92, 16\n\t"
    "s_lshl_b32 s93, s93, 12\n\t"
    "s_add_u32 s92, s92, %[kvb]\n\t"
    "s_add_u32 %[rn], %[rn], %[rdn]\n\t"
    "s_add_u32 s92, s92, s93\n\t"
    "buffer_load_dwordx4 v[160:163], %[vo], %[rk], s92 offen nt\n\t"
    "buffer_load_dwordx4 v[164:167], %[vo], %[rk], s92 offen offset:1024 nt\n\t"
    "buffer_load_dwordx4 v[168:171], %[vo], %[rk], s92 offen offset:2048 nt\n\t"
    "buffer_load_dwordx4 v[172:175], %[vo], %[rk], s92 offen offset:3072 nt\n\t"
    "buffer_load_dwordx4 v[176:179], %[vo], %[rv], s92 offen nt\n\t"
    "buffer_load_dwordx4 v[180:183], %[vo], %[rv], s92 offen offset:1024 nt\n\t"
    "buffer_load_dwordx4 v[184:187], %[vo], %[rv], s92 offen offset:2048 nt\n\t"
    "buffer_load_dwordx4 v[188:191], %[vo], %[rv], s92 offen offset:3072 nt\n\t"
    "s_waitcnt vmcnt(30)\n\t"
    "v_dot2_f32_bf16 v68, v192, v64, 0\n\t"
    "v_dot2_f32_bf16 v69, v196, v64, 0\n\t"
    "s_waitcnt vmcnt(28)\n\t"
    "v_dot2_f32_bf16 v70, v200, v64, 0\n\t"
    "v_dot2_f32_bf16 v71, v204, v64, 0\n\t"
    "v_dot2_f32_bf16 v68, v193, v65, v68\n\t"
    "v_dot2_f32_bf16 v69, v197, v65, v69\n\t"
    "v_dot2_f32_bf16 v70, v201, v65, v70\n\t"
    "v_dot2_f32_bf16 v71, v205, v65, v71\n\t"
    "v_dot2_f32_bf16 v68, v194, v66, v68\n\t"
    "v_dot2_f32_bf16 v69, v198, v66, v69\n\t"
    "v_dot2_f32_bf16 v70, v202, v66, v70\n\t"
    "v_dot2_f32_bf16 v71, v206, v66, v71\n\t"
    "v_dot2_f32_bf16 v68, v195, v67, v68\n\t"
    "v_dot2_f32_bf16 v69, v199, v67, v69\n\t"
    "v_dot2_f32_bf16 v70, v203, v67, v70\n\t"
    "v_dot2_f32_bf16 v71, v207, v67, v71\n\t"
    "v_add_f32_dpp v68, v68, v68 row_ror:8 row_mask:0xf bank_mask:0xf bound_ctrl:1\n\t"
    "v_add_f32_dpp v69, v69, v69 row_ror:8 row_mask:0xf bank_mask:0xf bound_ctrl:1\n\t"
    "v_add_f32_dpp v70, v70, v70 row_ror:8 row_mask:0xf bank_mask:0xf bound_ctrl:1\n\t"
    "v_add_f32_dpp v71, v71, v71 row_ror:8 row_mask:0xf bank_mask:0xf bound_ctrl:1\n\t"
    "v_cndmask_b32_e64 v72, v68, v70, s[88:89]\n\t"
    "v_cndmask_b32_e64 v73, v69, v71, s[88:89]\n\t"
    "s_nop 0\n\t"
    "v_add_f32_dpp v72, v72, v72 row_half_mirror row_mask:0xf bank_mask:0xf bound_ctrl:1\n\t"
    "v_add_f32_dpp v73, v73, v73 row_half_mirror row_mask:0xf bank_mask:0xf bound_ctrl:1\n\t"
    "v_cndmask_b32_e64 v74, v72, v73, s[90:91]\n\t"
    "s_nop 1\n\t"
    "v_add_f32_dpp v74, v74, v74 quad_perm:[2,3,0,1] row_mask:0xf bank_mask:0xf bound_ctrl:1\n\t"
    "s_nop 1\n\t"
    "v_add_f32_dpp v74, v74, v74 quad_perm:[1,0,3,2] row_mask:0xf bank_mask:0xf bound_ctrl:1\n\t"
    "v_cvt_pk_bf16_f32 v74, 0, v74\n\t"
    "v_mul_f32 v74, 0x3db504f3, v74\n\t"
    "v_cvt_pk_bf16_f32 v74, 0, v74\n\t"
    "s_nop 1\n\t"
    "v_max_f32_dpp v75, v74, v74 row_ror:4 row_mask:0xf bank_mask:0xf bound_ctrl:1\n\t"
    "s_nop 1\n\t"
    "v_max_f32_dpp v75, v75, v75 row_ror:8 row_mask:0xf bank_mask:0xf bound_ctrl:1\n\t"
    "v_mov_b32 v76, v75\n\t"
    "s_nop 1\n\t"
    "v_permlane16_swap_b32 v75, v76\n\t"
    "v_max_f32 v75, v75, v76\n\t"
    "v_mov_b32 v76, v75\n\t"
    "s_nop 1\n\t"
    "v_permlane32_swap_b32 v75, v76\n\t"
    "v_max3_f32 v75, v58, v75, v76\n\t"
    "v_sub_f32 v77, v74, v75\n\t"
    "v_sub_f32 v78, v58, v75\n\t"
    "v_mul_f32 v77, 0x3fb8aa3b, v77\n\t"
    "v_mul_f32 v78, 0x3fb8aa3b, v78\n\t"
    "v_exp_f32 v77, v77\n\t"
    "v_exp_f32 v78, v78\n\t"
    "v_mov_b32 v58, v75\n\t"
    "v_cvt_pk_bf16_f32 v76, 0, v77\n\t"
    "v_fma_f32 v59, v59, v78, v77\n\t"
    "v_sub_f32 v76, v77, v76\n\t"
    "v_cvt_pk_bf16_f32 v76, v77, v76\n\t"
    "v_pk_mul_f32 v[60:61], v[60:61], v[78:79] op_sel_hi:[1,0]\n\t"
    "v_pk_mul_f32 v[62:63], v[62:63], v[78:79] op_sel_hi:[1,0]\n\t"
    "v_mov_b32_dpp v68, v76 row_newbcast:0 row_mask:0xf bank_mask:0xf bound_ctrl:1\n\t"
    "v_mov_b32_dpp v69, v76 row_newbcast:4 row_mask:0xf bank_mask:0xf bound_ctrl:1\n\t"
    "v_mov_b32_dpp v70, v76 row_newbcast:8 row_mask:0xf bank_mask:0xf bound_ctrl:1\n\t"
    "v_mov_b32_dpp v71, v76 row_newbcast:12 row_mask:0xf bank_mask:0xf bound_ctrl:1\n\t"
    "v_perm_b32 v80, v68, v68, %[p0]\n\t"
    "v_perm_b32 v81, v68, v68, %[p1]\n\t"
    "v_perm_b32 v82, v68, v68, %[p2]\n\t"
    "v_perm_b32 v83, v68, v68, %[p3]\n\t"
    "v_perm_b32 v84, v69, v69, %[p0]\n\t"
    "v_perm_b32 v85, v69, v69, %[p1]\n\t"
    "v_perm_b32 v86, v69, v69, %[p2]\n\t"
    "v_perm_b32 v87, v69, v69, %[p3]\n\t"
    "v_perm_b32 v88, v70, v70, %[p0]\n\t"
    "v_perm_b32 v89, v70, v70, %[p1]\n\t"
    "v_perm_b32 v90, v70, v70, %[p2]\n\t"
    "v_perm_b32 v91, v70, v70, %[p3]\n\t"
    "v_perm_b32 v92, v71, v71, %[p0]\n\t"
    "v_perm_b32 v93, v71, v71, %[p1]\n\t"
    "v_perm_b32 v94, v71, v71, %[p2]\n\t"
    "v_perm_b32 v95, v71, v71, %[p3]\n\t"
    "s_waitcnt vmcnt(26)\n\t"
    "v_mfma_f32_16x16x32_bf16 v[60:63], v[80:83], v[208:211], v[60:63]\n\t"
    "v_mfma_f32_16x16x32_bf16 v[60:63], v[84:87], v[212:215], v[60:63]\n\t"
    "s_waitcnt vmcnt(24)\n\t"
    "v_mfma_f32_16x16x32_bf16 v[60:63], v[88:91], v[216:219], v[60:63]\n\t"
    "v_mfma_f32_16x16x32_bf16 v[60:63], v[92:95], v[220:223], v[60:63]\n\t"
    "s_bfe_u32 s92, %[rn], 0x50002\n\t"
    "s_and_b32 s93, %[rn], 3\n\t"
    "s_lshl_b32 s92, s92, 16\n\t"
    "s_lshl_b32 s93, s93, 12\n\t"
    "s_add_u32 s92, s92, %[kvb]\n\t"
    "s_add_u32 %[rn], %[rn], %[rdn]\n\t"
    "s_add_u32 s92, s92, s93\n\t"
    "buffer_load_dwordx4 v[192:195], %[vo], %[rk], s92 offen nt\n\t"
    "buffer_load_dwordx4 v[196:199], %[vo], %[rk], s92 offen offset:1024 nt\n\t"
    "buffer_load_dwordx4 v[200:203], %[vo], %[rk], s92 offen offset:2048 nt\n\t"
    "buffer_load_dwordx4 v[204:207], %[vo], %[rk], s92 offen offset:3072 nt\n\t"
    "buffer_load_dwordx4 v[208:211], %[vo], %[rv], s92 offen nt\n\t"
    "buffer_load_dwordx4 v[212:215], %[vo], %[rv], s92 offen offset:1024 nt\n\t"
    "buffer_load_dwordx4 v[216:219], %[vo], %[rv], s92 offen offset:2048 nt\n\t"
    "buffer_load_dwordx4 v[220:223], %[vo], %[rv], s92 offen offset:3072 nt\n\t"
    "s_cmp_lg_u32 %[it], %[sig]\n\t"
    "s_cbranch_scc1 31f\n\t"
    "s_mov_b64 exec, 1\n\t"
    "ds_add_u32 %[lc], v79\n\t"
    "s_mov_b64 exec, -1\n\t"
    "31:\n\t"
    "s_sub_u32 %[it], %[it], 1\n\t"
    "s_cmp_lg_u32 %[it], 0\n\t"
    "s_cbranch_scc1 30b\n\t"
    "40:\n\t"
    "s_waitcnt vmcnt(30)\n\t"
    "v_dot2_f32_bf16 v68, v96, v64, 0\n\t"
    "v_dot2_f32_bf16 v69, v100, v64, 0\n\t"
    "s_waitcnt vmcnt(28)\n\t"
    "v_dot2_f32_bf16 v70, v104, v64, 0\n\t"
    "v_dot2_f32_bf16 v71, v108, v64, 0\n\t"
    "v_dot2_f32_bf16 v68, v97, v65, v68\n\t"
    "v_dot2_f32_bf16 v69, v101, v65, v69\n\t"
    "v_dot2_f32_bf16 v70, v105, v65, v70\n\t"
    "v_dot2_f32_bf16 v71, v109, v65, v71\n\t"
    "v_dot2_f32_bf16 v68, v98, v66, v68\n\t"
    "v_dot2_f32_bf16 v69, v102, v66, v69\n\t"
    "v_dot2_f32_bf16 v70, v106, v66, v70\n\t"
    "v_dot2_f32_bf16 v71, v110, v66, v71\n\t"
    "v_dot2_f32_bf16 v68, v99, v67, v68\n\t"
    "v_dot2_f32_bf16 v69, v103, v67, v69\n\t"
    "v_dot2_f32_bf16 v70, v107, v67, v70\n\t"
    "v_dot2_f32_bf16 v71, v111, v67, v71\n\t"
    "v_add_f32_dpp v68, v68, v68 row_ror:8 row_mask:0xf bank_mask:0xf bound_ctrl:1\n\t"
    "v_add_f32_dpp v69, v69, v69 row_ror:8 row_mask:0xf bank_mask:0xf bound_ctrl:1\n\t"
    "v_add_f32_dpp v70, v70, v70 row_ror:8 row_mask:0xf bank_mask:0xf bound_ctrl:1\n\t"
    "v_add_f32_dpp v71, v71, v71 row_ror:8 row_mask:0xf bank_mask:0xf bound_ctrl:1\n\t"
    "v_cndmask_b32_e64 v72, v68, v70, s[88:89]\n\t"
    "v_cndmask_b32_e64 v73, v69, v71, s[88:89]\n\t"
    "s_nop 0\n\t"
    "v_add_f32_dpp v72, v72, v72 row_half_mirror row_mask:0xf bank_mask:0xf bound_ctrl:1\n\t"
    "v_add_f32_dpp v73, v73, v73 row_half_mirror row_mask:0xf bank_mask:0xf bound_ctrl:1\n\t"
    "v_cndmask_b32_e64 v74, v72, v73, s[90:91]\n\t"
    "s_nop 1\n\t"
    "v_add_f32_dpp v74, v74, v74 quad_perm:[2,3,0,1] row_mask:0xf bank_mask:0xf bound_ctrl:1\n\t"
    "s_nop 1\n\t"
    "v_add_f32_dpp v74, v74, v74 quad_perm:[1,0,3,2] row_mask:0xf bank_mask:0xf bound_ctrl:1\n\t"
    "v_cvt_pk_bf16_f32 v74, 0, v74\n\t"
    "v_mul_f32 v74, 0x3db504f3, v74\n\t"
    "v_cvt_pk_bf16_f32 v74, 0, v74\n\t"
    "s_nop 1\n\t"
    "v_max_f32_dpp v75, v74, v74 row_ror:4 row_mask:0xf bank_mask:0xf bound_ctrl:1\n\t"
    "s_nop 1\n\t"
    "v_max_f32_dpp v75, v75, v75 row_ror:8 row_mask:0xf bank_mask:0xf bound_ctrl:1\n\t"
    "v_mov_b32 v76, v75\n\t"
    "s_nop 1\n\t"
    "v_permlane16_swap_b32 v75, v76\n\t"
    "v_max_f32 v75, v75, v76\n\t"
    "v_mov_b32 v76, v75\n\t"
    "s_nop 1\n\t"
    "v_permlane32_swap_b32 v75, v76\n\t"
    "v_max3_f32 v75, v58, v75, v76\n\t"
    "v_sub_f32 v77, v74, v75\n\t"
    "v_sub_f32 v78, v58, v75\n\t"
    "v_mul_f32 v77, 0x3fb8aa3b, v77\n\t"
    "v_mul_f32 v78, 0x3fb8aa3b, v78\n\t"
    "v_exp_f32 v77, v77\n\t"
    "v_exp_f32 v78, v78\n\t"
    "v_mov_b32 v58, v75\n\t"
    "v_cvt_pk_bf16_f32 v76, 0, v77\n\t"
    "v_fma_f32 v59, v59, v78, v77\n\t"
    "v_sub_f32 v76, v77, v76\n\t"
    "v_cvt_pk_bf16_f32 v76, v77, v76\n\t"
    "v_pk_mul_f32 v[60:61], v[60:61], v[78:79] op_sel_hi:[1,0]\n\t"
    "v_pk_mul_f32 v[62:63], v[62:63], v[78:79] op_sel_hi:[1,0]\n\t"
    "v_mov_b32_dpp v68, v76 row_newbcast:0 row_mask:0xf bank_mask:0xf bound_ctrl:1\n\t"
    "v_mov_b32_dpp v69, v76 row_newbcast:4 row_mask:0xf bank_mask:0xf bound_ctrl:1\n\t"
    "v_mov_b32_dpp v70, v76 row_newbcast:8 row_mask:0xf bank_mask:0xf bound_ctrl:1\n\t"
    "v_mov_b32_dpp v71, v76 row_newbcast:12 row_mask:0xf bank_mask:0xf bound_ctrl:1\n\t"
    "v_perm_b32 v80, v68, v68, %[p0]\n\t"
    "v_perm_b32 v81, v68, v68, %[p1]\n\t"
    "v_perm_b32 v82, v68, v68, %[p2]\n\t"
    "v_perm_b32 v83, v68, v68, %[p3]\n\t"
    "v_perm_b32 v84, v69, v69, %[p0]\n\t"
    "v_perm_b32 v85, v69, v69, %[p1]\n\t"
    "v_perm_b32 v86, v69, v69, %[p2]\n\t"
    "v_perm_b32 v87, v69, v69, %[p3]\n\t"
    "v_perm_b32 v88, v70, v70, %[p0]\n\t"
    "v_perm_b32 v89, v70, v70, %[p1]\n\t"
    "v_perm_b32 v90, v70, v70, %[p2]\n\t"
    "v_perm_b32 v91, v70, v70, %[p3]\n\t"
    "v_perm_b32 v92, v71, v71, %[p0]\n\t"
    "v_perm_b32 v93, v71, v71, %[p1]\n\t"
    "v_perm_b32 v94, v71, v71, %[p2]\n\t"
    "v_perm_b32 v95, v71, v71, %[p3]\n\t"
    "s_waitcnt vmcnt(26)\n\t"
    "v_mfma_f32_16x16x32_bf16 v[60:63], v[80:83], v[112:115], v[60:63]\n\t"
    "v_mfma_f32_16x16x32_bf16 v[60:63], v[84:87], v[116:119], v[60:63]\n\t"
    "s_waitcnt vmcnt(24)\n\t"
    "v_mfma_f32_16x16x32_bf16 v[60:63], v[88:91], v[120:123], v[60:63]\n\t"
    "v_mfma_f32_16x16x32_bf16 v[60:63], v[92:95], v[124:127], v[60:63]\n\t"
    "s_waitcnt vmcnt(22)\n\t"
    "v_dot2_f32_bf16 v68, v128, v64, 0\n\t"
    "v_dot2_f32_bf16 v69, v132, v64, 0\n\t"
    "s_waitcnt vmcnt(20)\n\t"
    "v_dot2_f32_bf16 v70, v136, v64, 0\n\t"
    "v_dot2_f32_bf16 v71, v140, v64, 0\n\t"
    "v_dot2_f32_bf16 v68, v129, v65, v68\n\t"
    "v_dot2_f32_bf16 v69, v133, v65, v69\n\t"
    "v_dot2_f32_bf16 v70, v137, v65, v70\n\t"
    "v_dot2_f32_bf16 v71, v141, v65, v71\n\t"
    "v_dot2_f32_bf16 v68, v130, v66, v68\n\t"
    "v_dot2_f32_bf16 v69, v134, v66, v69\n\t"
    "v_dot2_f32_bf16 v70, v138, v66, v70\n\t"
    "v_dot2_f32_bf16 v71, v142, v66, v71\n\t"
    "v_dot2_f32_bf16 v68, v131, v67, v68\n\t"
    "v_dot2_f32_bf16 v69, v135, v67, v69\n\t"
    "v_dot2_f32_bf16 v70, v139, v67, v70\n\t"
    "v_dot2_f32_bf16 v71, v143, v67, v71\n\t"
    "v_add_f32_dpp v68, v68, v68 row_ror:8 row_mask:0xf bank_mask:0xf bound_ctrl:1\n\t"
    "v_add_f32_dpp v69, v69, v69 row_ror:8 row_mask:0xf bank_mask:0xf bound_ctrl:1\n\t"
    "v_add_f32_dpp v70, v70, v70 row_ror:8 row_mask:0xf bank_mask:0xf bound_ctrl:1\n\t"
    "v_add_f32_dpp v71, v71, v71 row_ror:8 row_mask:0xf bank_mask:0xf bound_ctrl:1\n\t"
    "v_cndmask_b32_e64 v72, v68, v70, s[88:89]\n\t"
    "v_cndmask_b32_e64 v73, v69, v71, s[88:89]\n\t"
    "s_nop 0\n\t"
    "v_add_f32_dpp v72, v72, v72 row_half_mirror row_mask:0xf bank_mask:0xf bound_ctrl:1\n\t"
    "v_add_f32_dpp v73, v73, v73 row_half_mirror row_mask:0xf bank_mask:0xf bound_ctrl:1\n\t"
    "v_cndmask_b32_e64 v74, v72, v73, s[90:91]\n\t"
    "s_nop 1\n\t"
    "v_add_f32_dpp v74, v74, v74 quad_perm:[2,3,0,1] row_mask:0xf bank_mask:0xf bound_ctrl:1\n\t"
    "s_nop 1\n\t"
    "v_add_f32_dpp v74, v74, v74 quad_perm:[1,0,3,2] row_mask:0xf bank_mask:0xf bound_ctrl:1\n\t"
    "v_cvt_pk_bf16_f32 v74, 0, v74\n\t"
    "v_mul_f32 v74, 0x3db504f3, v74\n\t"
    "v_cvt_pk_bf16_f32 v74, 0, v74\n\t"
    "s_nop 1\n\t"
    "v_max_f32_dpp v75, v74, v74 row_ror:4 row_mask:0xf bank_mask:0xf bound_ctrl:1\n\t"
    "s_nop 1\n\t"
    "v_max_f32_dpp v75, v75, v75 row_ror:8 row_mask:0xf bank_mask:0xf bound_ctrl:1\n\t"
    "v_mov_b32 v76, v75\n\t"
    "s_nop 1\n\t"
    "v_permlane16_swap_b32 v75, v76\n\t"
    "v_max_f32 v75, v75, v76\n\t"
    "v_mov_b32 v76, v75\n\t"
    "s_nop 1\n\t"
    "v_permlane32_swap_b32 v75, v76\n\t"
    "v_max3_f32 v75, v58, v75, v76\n\t"
    "v_sub_f32 v77, v74, v75\n\t"
    "v_sub_f32 v78, v58, v75\n\t"
    "v_mul_f32 v77, 0x3fb8aa3b, v77\n\t"
    "v_mul_f32 v78, 0x3fb8aa3b, v78\n\t"
    "v_exp_f32 v77, v77\n\t"
    "v_exp_f32 v78, v78\n\t"
    "v_mov_b32 v58, v75\n\t"
    "v_cvt_pk_bf16_f32 v76, 0, v77\n\t"
    "v_fma_f32 v59, v59, v78, v77\n\t"
    "v_sub_f32 v76, v77, v76\n\t"
    "v_cvt_pk_bf16_f32 v76, v77, v76\n\t"
    "v_pk_mul_f32 v[60:61], v[60:61], v[78:79] op_sel_hi:[1,0]\n\t"
    "v_pk_mul_f32 v[62:63], v[62:63], v[78:79] op_sel_hi:[1,0]\n\t"
    "v_mov_b32_dpp v68, v76 row_newbcast:0 row_mask:0xf bank_mask:0xf bound_ctrl:1\n\t"
    "v_mov_b32_dpp v69, v76 row_newbcast:4 row_mask:0xf bank_mask:0xf bound_ctrl:1\n\t"
    "v_mov_b32_dpp v70, v76 row_newbcast:8 row_mask:0xf bank_mask:0xf bound_ctrl:1\n\t"
    "v_mov_b32_dpp v71, v76 row_newbcast:12 row_mask:0xf bank_mask:0xf bound_ctrl:1\n\t"
    "v_perm_b32 v80, v68, v68, %[p0]\n\t"
    "v_perm_b32 v81, v68, v68, %[p1]\n\t"
    "v_perm_b32 v82, v68, v68, %[p2]\n\t"
    "v_perm_b32 v83, v68, v68, %[p3]\n\t"
    "v_perm_b32 v84, v69, v69, %[p0]\n\t"
    "v_perm_b32 v85, v69, v69, %[p1]\n\t"
    "v_perm_b32 v86, v69, v69, %[p2]\n\t"
    "v_perm_b32 v87, v69, v69, %[p3]\n\t"
    "v_perm_b32 v88, v70, v70, %[p0]\n\t"
    "v_perm_b32 v89, v70, v70, %[p1]\n\t"
    "v_perm_b32 v90, v70, v70, %[p2]\n\t"
    "v_perm_b32 v91, v70, v70, %[p3]\n\t"
    "v_perm_b32 v92, v71, v71, %[p0]\n\t"
    "v_perm_b32 v93, v71, v71, %[p1]\n\t"
    "v_perm_b32 v94, v71, v71, %[p2]\n\t"
    "v_perm_b32 v95, v71, v71, %[p3]\n\t"
    "s_waitcnt vmcnt(18)\n\t"
    "v_mfma_f32_16x16x32_bf16 v[60:63], v[80:83], v[144:147], v[60:63]\n\t"
    "v_mfma_f32_16x16x32_bf16 v[60:63], v[84:87], v[148:151], v[60:63]\n\t"
    "s_waitcnt vmcnt(16)\n\t"
    "v_mfma_f32_16x16x32_bf16 v[60:63], v[88:91], v[152:155], v[60:63]\n\t"
    "v_mfma_f32_16x16x32_bf16 v[60:63], v[92:95], v[156:159], v[60:63]\n\t"
    "s_waitcnt vmcnt(14)\n\t"
    "v_dot2_f32_bf16 v68, v160, v64, 0\n\t"
    "v_dot2_f32_bf16 v69, v164, v64, 0\n\t"
    "s_waitcnt vmcnt(12)\n\t"
    "v_dot2_f32_bf16 v70, v168, v64, 0\n\t"
    "v_dot2_f32_bf16 v71, v172, v64, 0\n\t"
    "v_dot2_f32_bf16 v68, v161, v65, v68\n\t"
    "v_dot2_f32_bf16 v69, v165, v65, v69\n\t"
    "v_dot2_f32_bf16 v70, v169, v65, v70\n\t"
    "v_dot2_f32_bf16 v71, v173, v65, v71\n\t"
    "v_dot2_f32_bf16 v68, v162, v66, v68\n\t"
    "v_dot2_f32_bf16 v69, v166, v66, v69\n\t"
    "v_dot2_f32_bf16 v70, v170, v66, v70\n\t"
    "v_dot2_f32_bf16 v71, v174, v66, v71\n\t"
    "v_dot2_f32_bf16 v68, v163, v67, v68\n\t"
    "v_dot2_f32_bf16 v69, v167, v67, v69\n\t"
    "v_dot2_f32_bf16 v70, v171, v67, v70\n\t"
    "v_dot2_f32_bf16 v71, v175, v67, v71\n\t"
    "v_add_f32_dpp v68, v68, v68 row_ror:8 row_mask:0xf bank_mask:0xf bound_ctrl:1\n\t"
    "v_add_f32_dpp v69, v69, v69 row_ror:8 row_mask:0xf bank_mask:0xf bound_ctrl:1\n\t"
    "v_add_f32_dpp v70, v70, v70 row_ror:8 row_mask:0xf bank_mask:0xf bound_ctrl:1\n\t"
    "v_add_f32_dpp v71, v71, v71 row_ror:8 row_mask:0xf bank_mask:0xf bound_ctrl:1\n\t"
    "v_cndmask_b32_e64 v72, v68, v70, s[88:89]\n\t"
    "v_cndmask_b32_e64 v73, v69, v71, s[88:89]\n\t"
    "s_nop 0\n\t"
    "v_add_f32_dpp v72, v72, v72 row_half_mirror row_mask:0xf bank_mask:0xf bound_ctrl:1\n\t"
    "v_add_f32_dpp v73, v73, v73 row_half_mirror row_mask:0xf bank_mask:0xf bound_ctrl:1\n\t"
    "v_cndmask_b32_e64 v74, v72, v73, s[90:91]\n\t"
    "s_nop 1\n\t"
    "v_add_f32_dpp v74, v74, v74 quad_perm:[2,3,0,1] row_mask:0xf bank_mask:0xf bound_ctrl:1\n\t"
    "s_nop 1\n\t"
    "v_add_f32_dpp v74, v74, v74 quad_perm:[1,0,3,2] row_mask:0xf bank_mask:0xf bound_ctrl:1\n\t"
    "v_cvt_pk_bf16_f32 v74, 0, v74\n\t"
    "v_mul_f32 v74, 0x3db504f3, v74\n\t"
    "v_cvt_pk_bf16_f32 v74, 0, v74\n\t"
    "s_nop 1\n\t"
    "v_max_f32_dpp v75, v74, v74 row_ror:4 row_mask:0xf bank_mask:0xf bound_ctrl:1\n\t"
    "s_nop 1\n\t"
    "v_max_f32_dpp v75, v75, v75 row_ror:8 row_mask:0xf bank_mask:0xf bound_ctrl:1\n\t"
    "v_mov_b32 v76, v75\n\t"
    "s_nop 1\n\t"
    "v_permlane16_swap_b32 v75, v76\n\t"
    "v_max_f32 v75, v75, v76\n\t"
    "v_mov_b32 v76, v75\n\t"
    "s_nop 1\n\t"
    "v_permlane32_swap_b32 v75, v76\n\t"
    "v_max3_f32 v75, v58, v75, v76\n\t"
    "v_sub_f32 v77, v74, v75\n\t"
    "v_sub_f32 v78, v58, v75\n\t"
    "v_mul_f32 v77, 0x3fb8aa3b, v77\n\t"
    "v_mul_f32 v78, 0x3fb8aa3b, v78\n\t"
    "v_exp_f32 v77, v77\n\t"
    "v_exp_f32 v78, v78\n\t"
    "v_mov_b32 v58, v75\n\t"
    "v_cvt_pk_bf16_f32 v76, 0, v77\n\t"
    "v_fma_f32 v59, v59, v78, v77\n\t"
    "v_sub_f32 v76, v77, v76\n\t"
    "v_cvt_pk_bf16_f32 v76, v77, v76\n\t"
    "v_pk_mul_f32 v[60:61], v[60:61], v[78:79] op_sel_hi:[1,0]\n\t"
    "v_pk_mul_f32 v[62:63], v[62:63], v[78:79] op_sel_hi:[1,0]\n\t"
    "v_mov_b32_dpp v68, v76 row_newbcast:0 row_mask:0xf bank_mask:0xf bound_ctrl:1\n\t"
    "v_mov_b32_dpp v69, v76 row_newbcast:4 row_mask:0xf bank_mask:0xf bound_ctrl:1\n\t"
    "v_mov_b32_dpp v70, v76 row_newbcast:8 row_mask:0xf bank_mask:0xf bound_ctrl:1\n\t"
    "v_mov_b32_dpp v71, v76 row_newbcast:12 row_mask:0xf bank_mask:0xf bound_ctrl:1\n\t"
    "v_perm_b32 v80, v68, v68, %[p0]\n\t"
    "v_perm_b32 v81, v68, v68, %[p1]\n\t"
    "v_perm_b32 v82, v68, v68, %[p2]\n\t"
    "v_perm_b32 v83, v68, v68, %[p3]\n\t"
    "v_perm_b32 v84, v69, v69, %[p0]\n\t"
    "v_perm_b32 v85, v69, v69, %[p1]\n\t"
    "v_perm_b32 v86, v69, v69, %[p2]\n\t"
    "v_perm_b32 v87, v69, v69, %[p3]\n\t"
    "v_perm_b32 v88, v70, v70, %[p0]\n\t"
    "v_perm_b32 v89, v70, v70, %[p1]\n\t"
    "v_perm_b32 v90, v70, v70, %[p2]\n\t"
    "v_perm_b32 v91, v70, v70, %[p3]\n\t"
    "v_perm_b32 v92, v71, v71, %[p0]\n\t"
    "v_perm_b32 v93, v71, v71, %[p1]\n\t"
    "v_perm_b32 v94, v71, v71, %[p2]\n\t"
    "v_perm_b32 v95, v71, v71, %[p3]\n\t"
    "s_waitcnt vmcnt(10)\n\t"
    "v_mfma_f32_16x16x32_bf16 v[60:63], v[80:83], v[176:179], v[60:63]\n\t"
    "v_mfma_f32_16x16x32_bf16 v[60:63], v[84:87], v[180:183], v[60:63]\n\t"
    "s_waitcnt vmcnt(8)\n\t"
    "v_mfma_f32_16x16x32_bf16 v[60:63], v[88:91], v[184:187], v[60:63]\n\t"
    "v_mfma_f32_16x16x32_bf16 v[60:63], v[92:95], v[188:191], v[60:63]\n\t"
    "s_setprio 3\n\t"
    "s_waitcnt vmcnt(6)\n\t"
    "v_dot2_f32_bf16 v68, v192, v64, 0\n\t"
    "v_dot2_f32_bf16 v69, v196, v64, 0\n\t"
    "s_waitcnt vmcnt(4)\n\t"
    "v_dot2_f32_bf16 v70, v200, v64, 0\n\t"
    "v_dot2_f32_bf16 v71, v204, v64, 0\n\t"
    "v_dot2_f32_bf16 v68, v193, v65, v68\n\t"
    "v_dot2_f32_bf16 v69, v197, v65, v69\n\t"
    "v_dot2_f32_bf16 v70, v201, v65, v70\n\t"
    "v_dot2_f32_bf16 v71, v205, v65, v71\n\t"
    "v_dot2_f32_bf16 v68, v194, v66, v68\n\t"
    "v_dot2_f32_bf16 v69, v198, v66, v69\n\t"
    "v_dot2_f32_bf16 v70, v202, v66, v70\n\t"
    "v_dot2_f32_bf16 v71, v206, v66, v71\n\t"
    "v_dot2_f32_bf16 v68, v195, v67, v68\n\t"
    "v_dot2_f32_bf16 v69, v199, v67, v69\n\t"
    "v_dot2_f32_bf16 v70, v203, v67, v70\n\t"
    "v_dot2_f32_bf16 v71, v207, v67, v71\n\t"
    "v_add_f32_dpp v68, v68, v68 row_ror:8 row_mask:0xf bank_mask:0xf bound_ctrl:1\n\t"
    "v_add_f32_dpp v69, v69, v69 row_ror:8 row_mask:0xf bank_mask:0xf bound_ctrl:1\n\t"
    "v_add_f32_dpp v70, v70, v70 row_ror:8 row_mask:0xf bank_mask:0xf bound_ctrl:1\n\t"
    "v_add_f32_dpp v71, v71, v71 row_ror:8 row_mask:0xf bank_mask:0xf bound_ctrl:1\n\t"
    "v_cndmask_b32_e64 v72, v68, v70, s[88:89]\n\t"
    "v_cndmask_b32_e64 v73, v69, v71, s[88:89]\n\t"
    "s_nop 0\n\t"
    "v_add_f32_dpp v72, v72, v72 row_half_mirror row_mask:0xf bank_mask:0xf bound_ctrl:1\n\t"
    "v_add_f32_dpp v73, v73, v73 row_half_mirror row_mask:0xf bank_mask:0xf bound_ctrl:1\n\t"
    "v_cndmask_b32_e64 v74, v72, v73, s[90:91]\n\t"
    "s_nop 1\n\t"
    "v_add_f32_dpp v74, v74, v74 quad_perm:[2,3,0,1] row_mask:0xf bank_mask:0xf bound_ctrl:1\n\t"
    "s_nop 1\n\t"
    "v_add_f32_dpp v74, v74, v74 quad_perm:[1,0,3,2] row_mask:0xf bank_mask:0xf bound_ctrl:1\n\t"
    "v_cvt_pk_bf16_f32 v74, 0, v74\n\t"
    "v_mul_f32 v74, 0x3db504f3, v74\n\t"
    "v_cvt_pk_bf16_f32 v74, 0, v74\n\t"
    "s_nop 1\n\t"
    "v_max_f32_dpp v75, v74, v74 row_ror:4 row_mask:0xf bank_mask:0xf bound_ctrl:1\n\t"
    "s_nop 1\n\t"
    "v_max_f32_dpp v75, v75, v75 row_ror:8 row_mask:0xf bank_mask:0xf bound_ctrl:1\n\t"
    "v_mov_b32 v76, v75\n\t"
    "s_nop 1\n\t"
    "v_permlane16_swap_b32 v75, v76\n\t"
    "v_max_f32 v75, v75, v76\n\t"
    "v_mov_b32 v76, v75\n\t"
    "s_nop 1\n\t"
    "v_permlane32_swap_b32 v75, v76\n\t"
    "v_max3_f32 v75, v58, v75, v76\n\t"
    "v_sub_f32 v77, v74, v75\n\t"
    "v_sub_f32 v78, v58, v75\n\t"
    "v_mul_f32 v77, 0x3fb8aa3b, v77\n\t"
    "v_mul_f32 v78, 0x3fb8aa3b, v78\n\t"
    "v_exp_f32 v77, v77\n\t"
    "v_exp_f32 v78, v78\n\t"
    "v_mov_b32 v58, v75\n\t"
    "v_cvt_pk_bf16_f32 v76, 0, v77\n\t"
    "v_fma_f32 v59, v59, v78, v77\n\t"
    "v_sub_f32 v76, v77, v76\n\t"
    "v_cvt_pk_bf16_f32 v76, v77, v76\n\t"
    "v_pk_mul_f32 v[60:61], v[60:61], v[78:79] op_sel_hi:[1,0]\n\t"
    "v_pk_mul_f32 v[62:63], v[62:63], v[78:79] op_sel_hi:[1,0]\n\t"
    "v_mov_b32_dpp v68, v76 row_newbcast:0 row_mask:0xf bank_mask:0xf bound_ctrl:1\n\t"
    "v_mov_b32_dpp v69, v76 row_newbcast:4 row_mask:0xf bank_mask:0xf bound_ctrl:1\n\t"
    "v_mov_b32_dpp v70, v76 row_newbcast:8 row_mask:0xf bank_mask:0xf bound_ctrl:1\n\t"
    "v_mov_b32_dpp v71, v76 row_newbcast:12 row_mask:0xf bank_mask:0xf bound_ctrl:1\n\t"
    "v_add_f32_dpp v72, v59, v59 row_ror:4 row_mask:0xf bank_mask:0xf bound_ctrl:1\n\t"
    "v_perm_b32 v80, v68, v68, %[p0]\n\t"
    "v_perm_b32 v81, v68, v68, %[p1]\n\t"
    "v_add_f32_dpp v72, v72, v72 row_ror:8 row_mask:0xf bank_mask:0xf bound_ctrl:1\n\t"
    "v_perm_b32 v82, v68, v68, %[p2]\n\t"
    "v_mov_b32 v73, v72\n\t"
    "v_perm_b32 v83, v68, v68, %[p3]\n\t"
    "v_perm_b32 v84, v69, v69, %[p0]\n\t"
    "v_permlane16_swap_b32 v72, v73\n\t"
    "v_add_f32 v72, v72, v73\n\t"
    "v_mov_b32 v73, v72\n\t"
    "v_perm_b32 v85, v69, v69, %[p1]\n\t"
    "v_perm_b32 v86, v69, v69, %[p2]\n\t"
    "v_permlane32_swap_b32 v72, v73\n\t"
    "v_add_f32 v59, v72, v73\n\t"
    "v_perm_b32 v87, v69, v69, %[p3]\n\t"
    "v_perm_b32 v88, v70, v70, %[p0]\n\t"
    "v_perm_b32 v89, v70, v70, %[p1]\n\t"
    "v_perm_b32 v90, v70, v70, %[p2]\n\t"
    "v_perm_b32 v91, v70, v70, %[p3]\n\t"
    "v_perm_b32 v92, v71, v71, %[p0]\n\t"
    "v_perm_b32 v93, v71, v71, %[p1]\n\t"
    "v_perm_b32 v94, v71, v71, %[p2]\n\t"
    "v_perm_b32 v95, v71, v71, %[p3]\n\t"
    "s_mov_b64 exec, 1\n\t"
    "ds_write_b64 %[lm], v[58:59]\n\t"
    "s_mov_b64 exec, -1\n\t"
    "s_waitcnt vmcnt(2)\n\t"
    "v_mfma_f32_16x16x32_bf16 v[60:63], v[80:83], v[208:211], v[60:63]\n\t"
    "v_mfma_f32_16x16x32_bf16 v[60:63], v[84:87], v[212:215], v[60:63]\n\t"
    "s_waitcnt vmcnt(0)\n\t"
    "v_mfma_f32_16x16x32_bf16 v[60:63], v[88:91], v[216:219], v[60:63]\n\t"
    "v_mfma_f32_16x16x32_bf16 v[60:63], v[92:95], v[220:223], v[60:63]\n\t"
    "s_nop 7\n\t"
    "46:\n\t"
    "ds_write_b128 %[lr], v[60:63]\n\t"
    "s_mov_b64 exec, 1\n\t"
    "ds_add_rtn_u32 v72, %[lc], v79 offset:4\n\t"
    "s_mov_b64 exec, -1\n\t"
    "ds_read_b64 v[74:75], %[l8]\n\t"
    "ds_read_b128 v[96:99], %[lb]\n\t"
    "ds_read_b128 v[100:103], %[lb] offset:1024\n\t"
    "ds_read_b128 v[104:107], %[lb] offset:2048\n\t"
    "ds_read_b128 v[108:111], %[lb] offset:3072\n\t"
    "ds_read_b128 v[112:115], %[lb] offset:4096\n\t"
    "ds_read_b128 v[116:119], %[lb] offset:5120\n\t"
    "ds_read_b128 v[120:123], %[lb] offset:6144\n\t"
    "ds_read_b128 v[124:127], %[lb] offset:7168\n\t"
    "s_waitcnt lgkmcnt(9)\n\t"
    "v_readfirstlane_b32 s92, v72\n\t"
    "s_mov_b32 s90, 0\n\t"
    "s_mov_b32 s91, -1\n\t"
    "s_cmp_lg_u32 s92, 7\n\t"
    "s_cbranch_scc1 50f\n\t"
    "s_waitcnt lgkmcnt(8)\n\t"
    "v_max_f32_dpp v76, v74, v74 quad_perm:[1,0,3,2] row_mask:0xf bank_mask:0xf bound_ctrl:1\n\t"
    "s_nop 1\n\t"
    "v_max_f32_dpp v76, v76, v76 quad_perm:[2,3,0,1] row_mask:0xf bank_mask:0xf bound_ctrl:1\n\t"
    "s_nop 1\n\t"
    "v_max_f32_dpp v76, v76, v76 row_half_mirror row_mask:0xf bank_mask:0xf bound_ctrl:1\n\t"
    "v_sub_f32 v77, v74, v76\n\t"
    "v_mul_f32 v77, 0x3fb8aa3b, v77\n\t"
    "v_exp_f32 v77, v77\n\t"
    "v_mov_b32 v79, 8\n\t"
    "v_mul_f32 v78, v77, v75\n\t"
    "v_mov_b32_dpp v80, v77 row_newbcast:0 row_mask:0xf bank_mask:0xf bound_ctrl:1\n\t"
    "v_mov_b32_dpp v82, v77 row_newbcast:1 row_mask:0xf bank_mask:0xf bound_ctrl:1\n\t"
    "v_add_f32_dpp v78, v78, v78 quad_perm:[1,0,3,2] row_mask:0xf bank_mask:0xf bound_ctrl:1\n\t"
    "v_mov_b32_dpp v84, v77 row_newbcast:2 row_mask:0xf bank_mask:0xf bound_ctrl:1\n\t"
    "v_mov_b32_dpp v86, v77 row_newbcast:3 row_mask:0xf bank_mask:0xf bound_ctrl:1\n\t"
    "v_add_f32_dpp v78, v78, v78 quad_perm:[2,3,0,1] row_mask:0xf bank_mask:0xf bound_ctrl:1\n\t"
    "v_mov_b32_dpp v88, v77 row_newbcast:4 row_mask:0xf bank_mask:0xf bound_ctrl:1\n\t"
    "v_mov_b32_dpp v90, v77 row_newbcast:5 row_mask:0xf bank_mask:0xf bound_ctrl:1\n\t"
    "v_add_f32_dpp v78, v78, v78 row_half_mirror row_mask:0xf bank_mask:0xf bound_ctrl:1\n\t"
    "v_mov_b32_dpp v92, v77 row_newbcast:6 row_mask:0xf bank_mask:0xf bound_ctrl:1\n\t"
    "v_mov_b32_dpp v94, v77 row_newbcast:7 row_mask:0xf bank_mask:0xf bound_ctrl:1\n\t"
    "s_waitcnt lgkmcnt(6)\n\t"
    "v_pk_mul_f32 v[60:61], v[96:97], v[80:81] op_sel_hi:[1,0]\n\t"
    "v_pk_mul_f32 v[62:63], v[98:99], v[80:81] op_sel_hi:[1,0]\n\t"
    "v_pk_mul_f32 v[64:65], v[100:101], v[82:83] op_sel_hi:[1,0]\n\t"
    "v_pk_mul_f32 v[66:67], v[102:103], v[82:83] op_sel_hi:[1,0]\n\t"
    "s_waitcnt lgkmcnt(4)\n\t"
    "v_pk_fma_f32 v[60:61], v[104:105], v[84:85], v[60:61] op_sel_hi:[1,0,1]\n\t"
    "v_pk_fma_f32 v[62:63], v[106:107], v[84:85], v[62:63] op_sel_hi:[1,0,1]\n\t"
    "v_pk_fma_f32 v[64:65], v[108:109], v[86:87], v[64:65] op_sel_hi:[1,0,1]\n\t"
    "v_pk_fma_f32 v[66:67], v[110:111], v[86:87], v[66:67] op_sel_hi:[1,0,1]\n\t"
    "s_waitcnt lgkmcnt(2)\n\t"
    "v_pk_fma_f32 v[60:61], v[112:113], v[88:89], v[60:61] op_sel_hi:[1,0,1]\n\t"
    "v_pk_fma_f32 v[62:63], v[114:115], v[88:89], v[62:63] op_sel_hi:[1,0,1]\n\t"
    "v_pk_fma_f32 v[64:65], v[116:117], v[90:91], v[64:65] op_sel_hi:[1,0,1]\n\t"
    "v_pk_fma_f32 v[66:67], v[118:119], v[90:91], v[66:67] op_sel_hi:[1,0,1]\n\t"
    "s_waitcnt lgkmcnt(0)\n\t"
    "v_pk_fma_f32 v[60:61], v[120:121], v[92:93], v[60:61] op_sel_hi:[1,0,1]\n\t"
    "v_pk_fma_f32 v[62:63], v[122:123], v[92:93], v[62:63] op_sel_hi:[1,0,1]\n\t"
    "v_pk_fma_f32 v[64:65], v[124:125], v[94:95], v[64:65] op_sel_hi:[1,0,1]\n\t"
    "v_pk_fma_f32 v[66:67], v[126:127], v[94:95], v[66:67] op_sel_hi:[1,0,1]\n\t"
    "v_pk_add_f32 v[60:61], v[60:61], v[64:65]\n\t"
    "v_pk_add_f32 v[62:63], v[62:63], v[66:67]\n\t"
    "v_mov_b32 v64, v60\n\t"
    "v_mov_b32 v65, v61\n\t"
    "v_mov_b32 v66, v62\n\t"
    "v_mov_b32 v67, v63\n\t"
    "s_nop 0\n\t"
    "v_permlane32_swap_b32 v60, v64\n\t"
    "v_permlane32_swap_b32 v61, v65\n\t"
    "v_permlane32_swap_b32 v62, v66\n\t"
    "v_permlane32_swap_b32 v63, v67\n\t"
    "v_pk_add_f32 v[60:61], v[60:61], v[64:65]\n\t"
    "v_pk_add_f32 v[62:63], v[62:63], v[66:67]\n\t"
    "v_cndmask_b32_e64 v60, v60, v76, s[90:91]\n\t"
    "v_cndmask_b32_e64 v61, v61, v78, s[90:91]\n\t"
    "v_min_u32 v62, -2, v62\n\t"
    "v_min_u32 v63, -2, v63\n\t"
    "v_min_u32 v60, -2, v60\n\t"
    "v_min_u32 v61, -2, v61\n\t"
    "buffer_store_dwordx4 v[60:63], %[ra], %[rw], 0 offen sc1\n\t"
    "s_mov_b64 exec, 1\n\t"
    "ds_add_u32 %[lc], v79\n\t"
    "s_mov_b64 exec, -1\n\t"
    "50:\n\t"
    "s_setprio 0\n\t"
    "s_waitcnt lgkmcnt(0)\n\t"
      : [rn] "+&s"(rn), [it] "+&s"(cyc)
      : [vo] "v"(vo), [lf] "v"(a_lf), [lc] "v"(a_lc), [lq] "v"(a_lq), [p0] "v"(psel[0]), [p1] "v"(psel[1]), [p2] "v"(psel[2]), [p3] "v"(psel[3]),
        [rk] "s"(dk), [rv] "s"(dv), [kvb] "s"(kvb), [rdn] "s"(rdn), [sig] "s"(sig), [wv] "s"(w),
        [lm] "v"(a_lm), [lr] "v"(a_lr), [l8] "v"(a_l8), [lb] "v"(a_lb), [ra] "v"(a_ra), [rw] "s"(dw)
      : "v58", "v59", "v60", "v61", "v62", "v63", "v64", "v65", "v66", "v67", "v68", "v69", "v70", "v71", "v72", "v73", "v74", "v75", "v76", "v77", "v78", "v79", "v80", "v81", "v82", "v83", "v84", "v85", "v86", "v87", "v88", "v89", "v90", "v91", "v92", "v93", "v94", "v95", "v96", "v97", "v98", "v99", "v100", "v101", "v102", "v103", "v104", "v105", "v106", "v107", "v108", "v109", "v110", "v111", "v112", "v113", "v114", "v115", "v116", "v117", "v118", "v119", "v120", "v121", "v122", "v123", "v124", "v125", "v126", "v127", "v128", "v129", "v130", "v131", "v132", "v133", "v134", "v135", "v136", "v137", "v138", "v139", "v140", "v141", "v142", "v143", "v144", "v145", "v146", "v147", "v148", "v149", "v150", "v151", "v152", "v153", "v154", "v155", "v156", "v157", "v158", "v159", "v160", "v161", "v162", "v163", "v164", "v165", "v166", "v167", "v168", "v169", "v170", "v171", "v172", "v173", "v174", "v175", "v176", "v177", "v178", "v179", "v180", "v181", "v182", "v183", "v184", "v185", "v186", "v187", "v188", "v189", "v190", "v191", "v192", "v193", "v194", "v195", "v196", "v197", "v198", "v199", "v200", "v201", "v202", "v203", "v204", "v205", "v206", "v207", "v208", "v209", "v210", "v211", "v212", "v213", "v214", "v215", "v216", "v217", "v218", "v219", "v220", "v221", "v222", "v223", "s88", "s89", "s90", "s91", "s92", "s93", "s94", "scc", "memory");
  }
  if (w < 3) CODE_PF();
  if (w == FW) {
    const u32x2 tg = *reinterpret_cast<const u32x2*>(lds + L_TAG);
    tag = (u32)__builtin_amdgcn_readfirstlane(tg[0]); tag_hi = (u32)__builtin_amdgcn_readfirstlane(tg[1]);
#pragma unroll 1
    for (u32 spins = 0; spins < SPIN_CAP; ++spins) {
      FENCE();
      const u32x4 f0 = ld16<AUX_SC1>(rws, WS_FLAG + vo, 0), f1 = ld16<AUX_SC1>(rws, WS_FLAG + 1024 + vo, 0);
      const bool ok = (f0[0] == tag) & (f0[1] == tag_hi) & (f0[2] == tag) & (f0[3] == tag_hi) & (f1[0] == tag) & (f1[1] == tag_hi) & (f1[2] == tag) & (f1[3] == tag_hi);
      if (__all(ok)) break;
      __builtin_amdgcn_s_sleep(4);
    }
    if (lane == 0) __hip_atomic_store(lds + L_CNT + 3, 1u, __ATOMIC_RELAXED, __HIP_MEMORY_SCOPE_WORKGROUP);
  }

  if (isW) {
    if (w < 4) {
      const rsrc_t ra = mk(pwo, NH * DH * DM * 2);
      const u32 va = (512 * wg + 128 * w + 8 * (lane >> 3)) * 1280 + ro * 128 + (lane & 7) * 16;
      const u32 rb = WS_R + (u32)((4 * wg + w) * 4) * 640u;
      const u32 vb = (c >> 3) * (128 * 640), va2 = vb + r * 64 + (c & 7) * 8, vm = vb + (c & 3) * 640 + 512;
      u32x2 a[4][2], ml;
      u32 pw = (L_CS + ((w * 2 + (r & 1)) * 2 + (r >> 1)) * 64 + 8 * (c & 7)) * 4;
      asm volatile("" : "+v"(pw));
#define R_LOAD() do { ml = ld8<AUX_SC1>(rws, vm, rb); \
        _Pragma("unroll") for (int k = 0; k < 4; ++k) { a[k][0] = ld8<AUX_SC1>(rws, va2 + k * 640, rb); a[k][1] = ld8<AUX_SC1>(rws, va2 + k * 640 + 256, rb); } } while (0)
#define R_FULL(ok) do { u32 mx = umax(ml[0], ml[1]); \
        _Pragma("unroll") for (int k = 0; k < 4; ++k) mx = umax(umax(mx, umax(a[k][0][0], a[k][0][1])), umax(a[k][1][0], a[k][1][1])); \
        ok = __all(mx != SENT); } while (0)
      LWAIT(3, 1u);
      LWAIT(0, 3u);
      u32x4 WA[8], WB[8], TA[8], TB[8];
#pragma unroll
      for (int t = 0; t < 8; ++t) WA[t] = ld16<AUX_NT>(ra, va, t * 1280);
#pragma unroll
      for (int t = 0; t < 8; ++t) WB[t] = ld16<AUX_NT>(ra, va, (64 + t) * 1280);
      FENCE(); SB();
      R_LOAD();
      FENCE(); SB();
      transpose8(WA, TA); transpose8(WB, TB);
      bool got;
      R_FULL(got);
      if (!got) {
#pragma unroll 1
        for (u32 spins = 0; spins < SPIN_CAP; ++spins) {
          FENCE();
          R_LOAD();
          R_FULL(got);
          if (got) break;
          __builtin_amdgcn_s_sleep(1);
        }
      }
      const float ms = asf(ml[0]), ls = asf(ml[1]);
      float M = fmaxf(ms, dppf<XOR1>(ms)); M = fmaxf(M, dppf<XOR2>(M));
      const float we = ex2((ms - M) * LOG2E);
      float L = we * ls; L += dppf<XOR1>(L); L += dppf<XOR2>(L);
      const float wn = we * __builtin_amdgcn_rcpf(L);
      const float w0 = dppf<0x00>(wn), w1 = dppf<0x55>(wn), w2 = dppf<0xAA>(wn), w3 = dppf<0xFF>(wn);
      const f32x2 oa = (w0 * asf2(a[0][0]) + w1 * asf2(a[1][0])) + (w2 * asf2(a[2][0]) + w3 * asf2(a[3][0]));
      const f32x2 ob = (w0 * asf2(a[0][1]) + w1 * asf2(a[1][1])) + (w2 * asf2(a[2][1]) + w3 * asf2(a[3][1]));
      const u32x4 af = quad4(pkbf(oa[0], oa[1])), af2 = quad4(pkbf(ob[0], ob[1]));
      f32x4 dd[8];
#pragma unroll
      for (int e = 0; e < 8; ++e) dd[e] = mfma(af, TA[e], f32x4{0.f, 0.f, 0.f, 0.f});
#pragma unroll
      for (int e = 0; e < 8; ++e) dd[e] = mfma(af2, TB[e], dd[e]);
      KEEP8(dd);
      if ((r & 1) == (c >> 3)) {
        float* pp = reinterpret_cast<float*>(reinterpret_cast<char*>(lds) + pw);
        *reinterpret_cast<f32x4*>(pp) = f32x4{dd[0][0], dd[1][0], dd[2][0], dd[3][0]};
        *reinterpret_cast<f32x4*>(pp + 4) = f32x4{dd[4][0], dd[5][0], dd[6][0], dd[7][0]};
      }
    }
    u32 ya = 0;
    if (w == 7) { ya = WS_Y + ((lane >> 5) * 8 + wg) * 2560 + ro * 256 + (lane & 31) * 8; asm volatile("" : "+v"(ya)); }
    BAR();
    if (w == 7) {
      f32x2 y2[8];
#pragma unroll
      for (int k = 0; k < 8; ++k) y2[k] = *reinterpret_cast<const f32x2*>(ldf + L_CS + k * 128 + lane * 2);
      const f32x2 ys = ((y2[0] + y2[1]) + (y2[2] + y2[3])) + ((y2[4] + y2[5]) + (y2[6] + y2[7]));
      st8(rws, ya, u32x2{clean(ys[0]), clean(ys[1])});
    }
  } else if (isG) {
    u32x4 TA[8], TB[8], TC[8], WA[8], WB[8], WC[8];
    u32 xp = 0;
    LWAIT(0, w < 5 ? 3u : 11u);
    if (w < 5) {
      const rsrc_t ra = mk(pwg, DM * FF * 2), rb = mk(pwu, DM * FF * 2);
      const u32 va = (128 * w + 8 * (lane >> 2)) * 3456 + gc * 64 + (lane & 3) * 16;
#pragma unroll
      for (int t = 0; t < 8; ++t) WA[t] = ld16<0>(ra, va, t * 3456);
#pragma unroll
      for (int t = 0; t < 8; ++t) WB[t] = ld16<0>(rb, va, t * 3456);
      xp = __builtin_amdgcn_raw_buffer_load_b32(rx, gb * 1280 + w * 256 + lane * 4, 0, 0);
    }
    FENCE();
    if (w >= 3) {
      const rsrc_t rc = mk(pwd, FF * DM * 2);
      const u32 vc = (32 * gc + 8 * r) * 1280 + (w - 3) * 256 + c * 16;
#pragma unroll
      for (int t = 0; t < 8; ++t) WC[t] = ld16<0>(rc, vc, t * 1280);
    }
    FENCE();
#define GU_MFMA(AF) f32x4 da[8], db[8]; \
      _Pragma("unroll") for (int e = 0; e < 8; ++e) da[e] = mfma(AF, TA[e], f32x4{0.f, 0.f, 0.f, 0.f}); \
      _Pragma("unroll") for (int e = 0; e < 8; ++e) db[e] = mfma(AF, TB[e], f32x4{0.f, 0.f, 0.f, 0.f});
#define GU_STORE(DST) KEEP8(da); KEEP8(db); if (r == (c >> 2)) { float* pp = reinterpret_cast<float*>(reinterpret_cast<char*>(lds) + pg) + (DST); \
        *reinterpret_cast<f32x4*>(pp) = f32x4{da[0][0], da[1][0], da[2][0], da[3][0]}; \
        *reinterpret_cast<f32x4*>(pp + 4) = f32x4{da[4][0], da[5][0], da[6][0], da[7][0]}; \
        *reinterpret_cast<f32x4*>(pp + 32) = f32x4{db[0][0], db[1][0], db[2][0], db[3][0]}; \
        *reinterpret_cast<f32x4*>(pp + 36) = f32x4{db[4][0], db[5][0], db[6][0], db[7][0]}; }
    u32 pg = ((w * 4 + r) * 64 + 8 * (c & 3)) * 4, ps = (L_ST + 2 * w + r) * 4;
    asm volatile("" : "+v"(pg), "+v"(ps));
    if (w < 5) {
      transpose8(WA, TA); transpose8(WB, TB);
      const u32x4 ones = {0x3f803f80u, 0x3f803f80u, 0x3f803f80u, 0x3f803f80u};
      GU_MFMA(ones) GU_STORE(L_CS)
    }
    BAR();
    const int pc = (c >> 3) * 32 + 8 * r + (c & 7);
    float cs = 0.f;
    const bool odd = r & 1;
    u32 fo = lane < 32 ? WS_FP + (gb * 54 + gc) * 2560 + (w - 3) * 512 + c * 32 + r * 16 : OOB;
    if (w >= 3) {
#pragma unroll
      for (int k = 0; k < 20; ++k) cs += ldf[L_CS + k * 64 + pc];
      transpose8(WC, TC);
      asm volatile("" : "+v"(cs), "+v"(fo));
    }
    if (w < 5) {
      u32 yb0 = WS_Y + gb * 8 * 2560 + w * 512, yb1 = yb0 + 2 * 2560, yb2 = yb0 + 4 * 2560, yb3 = yb0 + 6 * 2560;
      asm volatile("" : "+s"(yb0), "+s"(yb1), "+s"(yb2), "+s"(yb3));
      const u32 yv = lane * 8;
      f32x2 xf = {blo(xp), bhi(xp)};
      asm volatile("" : "+v"(xf));
      u32x2 y[8];
#pragma unroll 1
      for (u32 spins = 0; spins < SPIN_CAP; ++spins) {
        FENCE();
        y[0] = ld8<AUX_SC1>(rws, yv, yb0); y[1] = ld8<AUX_SC1>(rws, yv + 2560, yb0); y[2] = ld8<AUX_SC1>(rws, yv, yb1); y[3] = ld8<AUX_SC1>(rws, yv + 2560, yb1);
        y[4] = ld8<AUX_SC1>(rws, yv, yb2); y[5] = ld8<AUX_SC1>(rws, yv + 2560, yb2); y[6] = ld8<AUX_SC1>(rws, yv, yb3); y[7] = ld8<AUX_SC1>(rws, yv + 2560, yb3);
        u32 mx = umax(y[0][0], y[0][1]);
#pragma unroll
        for (int g = 1; g < 8; ++g) mx = umax(mx, umax(y[g][0], y[g][1]));
        if (__all(mx != SENT)) break;
        __builtin_amdgcn_s_sleep(1);
      }
      const f32x2 s2 = ((asf2(y[0]) + asf2(y[1])) + (asf2(y[2]) + asf2(y[3]))) + ((asf2(y[4]) + asf2(y[5])) + (asf2(y[6]) + asf2(y[7])));
      const f32x2 hs = xf + f32x2{RB(s2[0]), RB(s2[1])};
      const u32 hp = pkbf(hs[0], hs[1]);
      const u32x4 af = quad4(hp);
      GU_MFMA(af)
      const float h0 = blo(hp), h1 = bhi(hp);
      float sw = h0 + h1, sq = fmaf(h0, h0, h1 * h1);
      sw = rowsum(sw); sq = rowsum(sq);
      { const auto t = __builtin_amdgcn_permlane16_swap(asu(sw), asu(sq), false, false); sw = asf(t[0]) + asf(t[1]); }
      sw = sum32(sw);
      if ((lane & 47) == 0) *reinterpret_cast<float*>(reinterpret_cast<char*>(lds) + ps) = sw;
      GU_STORE(L_MG)
    }
    BAR();
    if (w >= 3) {
      const f32x4 t0 = *reinterpret_cast<const f32x4*>(ldf + L_ST), t1 = *reinterpret_cast<const f32x4*>(ldf + L_ST + 4);
      const f32x2 t2 = *reinterpret_cast<const f32x2*>(ldf + L_ST + 8);
      f32x2 pv2[10];
#pragma unroll
      for (int k = 0; k < 10; ++k) pv2[k] = f32x2{ldf[L_MG + (2 * k) * 64 + pc], ldf[L_MG + (2 * k + 1) * 64 + pc]};
      const f32x2 mq = (((f32x2{t0[0], t0[1]} + f32x2{t0[2], t0[3]}) + (f32x2{t1[0], t1[1]} + f32x2{t1[2], t1[3]})) + t2) * (1.f / DM);
      const float var = fmaxf(fmaf(-mq[0], mq[0], mq[1]), 0.f);
      const float rinv = __builtin_amdgcn_rcpf(RB(__builtin_amdgcn_sqrtf(RB(RB(var) + 1e-5f))));
      const f32x2 ss = (((pv2[0] + pv2[1]) + (pv2[2] + pv2[3])) + ((pv2[4] + pv2[5]) + (pv2[6] + pv2[7]))) + (pv2[8] + pv2[9]);
      const float gu = RB(rinv * fmaf(-RB(mq[0]), cs, ss[0] + ss[1]));
      const float ot = dppf<ROR8>(gu);
      const float g = (c & 8) ? ot : gu, u = (c & 8) ? gu : ot;
      const float z = (-2.f * LOG2E * 0.7978845608028654f) * fmaf(0.044715f * g * g, g, g);
      const float act = RB(g * __builtin_amdgcn_rcpf(1.f + ex2(z))) * u;
      const u32 ap = pkbf(act, dppf<XOR1>(act));
      const u32x4 af = {dppu<0x150>(ap), dppu<0x152>(ap), dppu<0x154>(ap), dppu<0x156>(ap)};
      f32x4 dc[8];
#pragma unroll
      for (int e = 0; e < 8; ++e) dc[e] = mfma(af, TC[e], f32x4{0.f, 0.f, 0.f, 0.f});
      KEEP8(dc);
      f32x4 o4;
#pragma unroll
      for (int v = 0; v < 4; ++v) o4[v] = odd ? dc[4 + v][0] : dc[v][0];
      st16(rws, fo, clean4(o4));
    }
  } else if (isF) {
    if (w == FW) {
      const int fu = (ro - 26) * 8 + xcd, fb = fu / 20, fc = fu - 20 * fb;
      const int ug = lane >> 3, q8 = lane & 7;
      const int oc = fb * 640 + fc * 32 + 4 * q8 + 2 * ((lane >> 3) & 1) + ((lane >> 4) & 1);
      const u32 xq = __builtin_amdgcn_raw_buffer_load_b16(rx, oc * 2, 0, 0);
      u32x4 y;
#pragma unroll 1
      for (u32 spins = 0; spins < SPIN_CAP; ++spins) {
        FENCE();
        y = ld16<AUX_SC1>(rws, WS_Y + (fb * 8 + ug) * 2560 + fc * 128 + q8 * 16, 0);
        if (__all(max4(y) != SENT)) break;
        __builtin_amdgcn_s_sleep(2);
      }
      float h = RB(blo(xq) + RB(red8(asf4(y), lane)));
      u32 oa = lane < 32 ? (u32)(oc * 2) : OOB;
      u32 fa = WS_FP + (fb * 54 + ug) * 2560 + fc * 128 + q8 * 16;
      u32 fa6 = ug < 6 ? fa : OOB;
      asm volatile("" : "+v"(h), "+v"(oa), "+v"(fa), "+v"(fa6));
      if (fu == 0) st8(rws, lane == 0 ? WS_EPOCH : OOB, u32x2{tag, tag_hi});
      u32x4 p[7];
#pragma unroll 1
      for (u32 spins = 0; spins < SPIN_CAP; ++spins) {
        FENCE();
#pragma unroll
        for (int k = 0; k < 7; ++k) p[k] = ld16<AUX_SC1>(rws, k < 6 ? fa : fa6, k * 8 * 2560);
        u32 mx = max4(p[0]);
#pragma unroll
        for (int k = 1; k < 7; ++k) mx = umax(mx, max4(p[k]));
        if (__all(mx != SENT)) break;
      }
      const f32x4 s4 = ((asf4(p[0]) + asf4(p[1])) + (asf4(p[2]) + asf4(p[3]))) + ((asf4(p[4]) + asf4(p[5])) + asf4(p[6]));
      __builtin_amdgcn_raw_buffer_store_b16((unsigned short)pkbf(h + RB(red8(s4, lane)), 0.f), mk(pout, 2 * DM * 2), oa, 0, AUX_SC1);
    }
  }
}
}

extern "C" void kernel_launch(void* const* d_in, const int* in_sizes, int n_in, void* d_out, int out_size, void* d_ws, size_t ws_size,
                              hipStream_t stream) {
  hipLaunchKernelGGL(blk::blk_k_block, dim3(256), dim3(512), 0, stream, (const void*)d_in[0], (const void*)d_in[1], (const void*)d_in[2],
                     (const void*)d_in[3], (const void*)d_in[4], (const void*)d_in[6], (const void*)d_in[7], (const void*)d_in[8], d_out, d_ws);
}
```
